```python
import torch

def _softmax(x, axis=-1):
    return torch.softmax(x, dim=axis)
def _gelu(x):
    return torch.nn.functional.gelu(x, approximate="tanh")
def _silu(x):
    return torch.nn.functional.silu(x)
def _sigmoid(x):
    return torch.sigmoid(x)
def _layernorm(x, axis=-1, eps=1e-5):
    m = x.mean(dim=axis, keepdim=True)
    v = x.var(dim=axis, keepdim=True, unbiased=False)
    return (x - m) / torch.sqrt(v + eps)
def _scatter_add(tgt, idx, src, axis=0):
    return tgt.clone().index_add_(axis, idx, src)
def _segment_sum(src, seg, n):
    out = torch.zeros((n,) + src.shape[1:], dtype=src.dtype)
    return out.index_add_(0, seg, src)
def _pad(x, pw):
    flat = tuple(v for pair in reversed(pw) for v in pair)
    return torch.nn.functional.pad(x, flat)

kernel_name = 'sequential_preLN__sliding_block_cached__dense_activation_gelu__decode'

def setup_inputs():

    _w = (lambda a: (lambda t: t.to(torch.bfloat16) if t.is_floating_point() else t)(torch.as_tensor(a)))
    def _A_setup():
        B, Sq, D = 1, 1, 512
        Hq, Hkv, Sk, Dh = 32, 8, 65536, 128
        assert Hq % Hkv == 0, (Hq, Hkv)
        return {
            "x": _w((lambda *s: torch.randn(*s, dtype=torch.bfloat16) * 0.1)(B, Sq, D)),
            "k_cache": _w((lambda *s: torch.randn(*s, dtype=torch.bfloat16) * 1.6)(B, Hkv, Sk, Dh)),
            "v_cache": _w((lambda *s: torch.randn(*s, dtype=torch.bfloat16) * 1.6)(B, Hkv, Sk, Dh)),
            "wq": _w((lambda *s: torch.randn(*s, dtype=torch.bfloat16) * 1.6)(D, Hq * Dh) * (D ** -0.5)),
            "wo": _w((lambda *s: torch.randn(*s, dtype=torch.bfloat16) * 1.6)(Hq * Dh, D) * ((Hq * Dh) ** -0.5)),
        }
    _A_in = _A_setup()

    def _B_setup():
        B, S, D, F = 1, 1, 512, 1792
        return {
            "x": _w((lambda *s: torch.randn(*s, dtype=torch.bfloat16) * 0.1)(B, S, D)),
            "w1": _w((lambda *s: torch.randn(*s, dtype=torch.bfloat16) * 3.2)(D, F) * (D ** -0.5)),
            "b1": _w((lambda *s: torch.randn(*s, dtype=torch.bfloat16) * 3.2)(F) * 0.01),
            "w2": _w((lambda *s: torch.randn(*s, dtype=torch.bfloat16) * 3.2)(F, D) * (F ** -0.5)),
            "b2": _w((lambda *s: torch.randn(*s, dtype=torch.bfloat16) * 3.2)(D) * 0.01),
        }
    _B_in = _B_setup()

    return {
        "A_x": _A_in["x"],
        "A_k_cache": _A_in["k_cache"],
        "A_v_cache": _A_in["v_cache"],
        "A_wq": _A_in["wq"],
        "A_wo": _A_in["wo"],
        "B_x": _B_in["x"],
        "B_w1": _B_in["w1"],
        "B_b1": _B_in["b1"],
        "B_w2": _B_in["w2"],
        "B_b2": _B_in["b2"],
    }

def reference(A_x, A_k_cache, A_v_cache, A_wq, A_wo, B_x, B_w1, B_b1, B_w2, B_b2):
    def _A_ref(x, k_cache, v_cache, wq, wo):
        B, Sq, D = x.shape
        Hq, Dh = 32, 128
        Hkv, Sk = k_cache.shape[1], k_cache.shape[2]
        W = min(4096, max(256, Sk // 4))
        rep = Hq // Hkv
        q = (lambda x, *d: x.permute(*d))((x @ wq).reshape(B, Sq, Hq, Dh), 0, 2, 1, 3)
        k = (lambda x, n, axis: torch.repeat_interleave(x, n, dim=axis))(k_cache[:, :, Sk - W:, :], rep, axis=1)
        v = (lambda x, n, axis: torch.repeat_interleave(x, n, dim=axis))(v_cache[:, :, Sk - W:, :], rep, axis=1)
        scores = torch.einsum("bhqd,bhkd->bhqk", q, k) * (Dh ** -0.5)
        p = _softmax(scores, axis=-1)
        o = torch.einsum("bhqk,bhkd->bhqd", p, v)
        return (lambda x, *d: x.permute(*d))(o, 0, 2, 1, 3).reshape(B, Sq, Hq * Dh) @ wo

    def _B_ref(x, w1, b1, w2, b2):
        h = torch.einsum("bsd,df->bsf", x, w1) + b1
        h = _gelu(h)
        return torch.einsum("bsf,fd->bsd", h, w2) + b2

    x = A_x
    h = x + _A_ref(_layernorm(x, axis=-1), A_k_cache, A_v_cache, A_wq, A_wo)
    h2 = h + _B_ref(_layernorm(h, axis=-1), B_w1, B_b1, B_w2, B_b2)
    return h2
```

```cpp
#include <hip/hip_runtime.h>
#include <hip/hip_bf16.h>
#include <stdint.h>

typedef unsigned short u16;
typedef unsigned v4u __attribute__((ext_vector_type(4)));

__device__ __forceinline__ float bf2f(unsigned b) { return __builtin_bit_cast(float, b << 16); }
__device__ __forceinline__ float rbf(float f) {
    unsigned u = __builtin_bit_cast(unsigned, f);
    u = (u + 0x7fffu + ((u >> 16) & 1u)) & 0xffff0000u;
    return __builtin_bit_cast(float, u);
}
__device__ __forceinline__ float wave_sum(float v) {
    for (int o = 32; o > 0; o >>= 1) v += __shfl_xor(v, o);
    return v;
}
__device__ __forceinline__ float wave_max(float v) {
    for (int o = 32; o > 0; o >>= 1) v = fmaxf(v, __shfl_xor(v, o));
    return v;
}

constexpr int D = 512, HQ = 32, HKV = 8, SK = 65536, DH = 128, W = 4096, F = 1792;

__device__ void ln512(const float* xs, float* out, int lane) {
    float s = 0.f;
    for (int i = lane; i < 512; i += 64) s += xs[i];
    s = wave_sum(s);
    float mean = s * (1.f / 512.f);
    float vs = 0.f;
    for (int i = lane; i < 512; i += 64) { float d = xs[i] - mean; vs += d * d; }
    vs = wave_sum(vs);
    float m = rbf(mean), v = rbf(vs * (1.f / 512.f));
    float sd = rbf(sqrtf(rbf(v + 1e-5f)));
    for (int i = lane; i < 512; i += 64) out[i] = rbf(rbf(xs[i] - m) / sd);
}

__global__ void k_q(const u16* x, const u16* wq, float* q) {
    __shared__ float xs[512], xn[512];
    int t = threadIdx.x;
    for (int i = t; i < 512; i += 64) xs[i] = bf2f(x[i]);
    __syncthreads();
    ln512(xs, xn, t);
    __syncthreads();
    int c = blockIdx.x * 64 + t;
    float a = 0.f;
    for (int i = 0; i < 512; ++i) a += xn[i] * bf2f(wq[(size_t)i * 4096 + c]);
    q[c] = rbf(a);
}
__global__ void k_attn(const float* q, const u16* kc, const u16* vc, float* part) {
    __shared__ float qs[128], ps[128], red[4];
    int s = blockIdx.x, h = blockIdx.y, g = h / 4, t = threadIdx.x, lane = t & 63, w = t >> 6;
    qs[t] = q[h * 128 + t];
    __syncthreads();
    size_t key = (size_t)g * SK + (SK - W) + s * 128 + t;
    const u16* kr = kc + key * 128;
    float a = 0.f;
    for (int d = 0; d < 128; ++d) a += qs[d] * bf2f(kr[d]);
    float sc = rbf(rbf(a) * 0.08838834764831845f);
    float m = wave_max(sc);
    if (lane == 0) red[w] = m;
    __syncthreads();
    m = fmaxf(red[0], red[1]);
    float p = __expf(sc - m);
    ps[t] = p;
    float l = wave_sum(p);
    if (lane == 0) red[2 + w] = l;
    __syncthreads();
    l = red[2] + red[3];
    const u16* vb = vc + ((size_t)g * SK + (SK - W) + s * 128) * 128;
    float o = 0.f;
    for (int j = 0; j < 128; ++j) o += ps[j] * bf2f(vb[(size_t)j * 128 + t]);
    float* pr = part + ((size_t)h * 32 + s) * 130;
    pr[t] = o;
    if (t == 0) { pr[128] = m; pr[129] = l; }
}
__global__ void k_comb(const float* part, float* o) {
    int h = blockIdx.x, t = threadIdx.x;
    float M = -INFINITY;
    for (int s = 0; s < 32; ++s) M = fmaxf(M, part[((size_t)h * 32 + s) * 130 + 128]);
    float L = 0.f, a = 0.f;
    for (int s = 0; s < 32; ++s) { const float* pr = part + ((size_t)h * 32 + s) * 130; float w = __expf(pr[128] - M); L += w * pr[129]; a += w * pr[t]; }
    o[h * 128 + t] = rbf(a / L);
}
__global__ void k_wo(const float* o, const u16* wo, float* pw) {
    int b = blockIdx.x, c = threadIdx.x;
    float a = 0.f;
    for (int r = 0; r < 64; ++r) a += o[b * 64 + r] * bf2f(wo[(size_t)(b * 64 + r) * 512 + c]);
    pw[b * 512 + c] = a;
}
__global__ void k_mlp1(const u16* x, const float* pw, const u16* w1, const u16* b1, float* hbuf, float* g) {
    __shared__ float hs[512], hn[512];
    int t = threadIdx.x;
    for (int c = t; c < 512; c += 64) { float a = 0.f; for (int b = 0; b < 64; ++b) a += pw[b * 512 + c]; hs[c] = rbf(bf2f(x[c]) + rbf(a)); if (blockIdx.x == 0) hbuf[c] = hs[c]; }
    __syncthreads();
    ln512(hs, hn, t);
    __syncthreads();
    int f = blockIdx.x * 64 + t;
    float a = 0.f;
    for (int i = 0; i < 512; ++i) a += hn[i] * bf2f(w1[(size_t)i * F + f]);
    float u = rbf(rbf(a) + bf2f(b1[f]));
    float th = tanhf(0.7978845608028654f * (u + 0.044715f * u * u * u));
    g[f] = rbf(0.5f * u * (1.f + th));
}
__global__ void k_mlp2(const float* g, const u16* w2, const u16* b2, const float* hbuf, u16* out) {
    int c = blockIdx.x * 64 + threadIdx.x;
    float a = 0.f;
    for (int f = 0; f < F; ++f) a += g[f] * bf2f(w2[(size_t)f * 512 + c]);
    float y = rbf(rbf(a) + bf2f(b2[c]));
    float r = rbf(hbuf[c] + y);
    out[c] = (u16)(__builtin_bit_cast(unsigned, r) >> 16);
}

extern "C" void kernel_launch(void* const* d_in, const int* in_sizes, int n_in, void* d_out, int out_size, void* d_ws, size_t ws_size, hipStream_t stream) {
    const u16* x = (const u16*)d_in[0]; const u16* kc = (const u16*)d_in[1]; const u16* vc = (const u16*)d_in[2];
    const u16* wq = (const u16*)d_in[3]; const u16* wo = (const u16*)d_in[4];
    const u16* w1 = (const u16*)d_in[6]; const u16* b1 = (const u16*)d_in[7]; const u16* w2 = (const u16*)d_in[8]; const u16* b2 = (const u16*)d_in[9];
    float* ws = (float*)d_ws;
    float* q = ws; float* part = ws + 4096; float* o = part + 32 * 32 * 130; float* pw = o + 4096; float* hb = pw + 64 * 512; float* g = hb + 512;
    k_q<<<64, 64, 0, stream>>>(x, wq, q);
    k_attn<<<dim3(32, 32), 128, 0, stream>>>(q, kc, vc, part);
    k_comb<<<32, 128, 0, stream>>>(part, o);
    k_wo<<<64, 512, 0, stream>>>(o, wo, pw);
    k_mlp1<<<28, 64, 0, stream>>>(x, pw, w1, b1, hb, g);
    k_mlp2<<<8, 64, 0, stream>>>(g, w2, b2, hb, (u16*)d_out);
}
```

```cpp
#include <hip/hip_runtime.h>
#include <stdint.h>

typedef unsigned short u16;
typedef unsigned v4u __attribute__((ext_vector_type(4)));
typedef unsigned v2u __attribute__((ext_vector_type(2)));
typedef float v4f __attribute__((ext_vector_type(4)));
typedef float v2f __attribute__((ext_vector_type(2)));
typedef __bf16 bf16x2 __attribute__((ext_vector_type(2)));
#define GAS __attribute__((address_space(1)))
typedef GAS unsigned long long gu64;
typedef GAS unsigned gu32;
#define RLX_AGENT __ATOMIC_RELAXED, __HIP_MEMORY_SCOPE_AGENT

constexpr int SK = 65536, WIN = 4096, FF = 1792;
constexpr int NWG = 256, NTHR = 576;
constexpr unsigned WS_EPOCH = 0;
constexpr unsigned WS_E1 = 4096;
constexpr unsigned WS_E2O = WS_E1 + 8 * 256 * 8;
constexpr unsigned WS_E2ML = WS_E2O + 8 * 32 * 32 * 16 * 8;
constexpr unsigned WS_E3A = WS_E2ML + 8 * 4 * 32 * 2 * 8;
constexpr unsigned WS_E3B = WS_E3A + 256 * 256 * 2 * 8;
constexpr unsigned WS_E5 = WS_E3B + 8 * 256 * 8;
constexpr unsigned WS_END = WS_E5 + 256 * 256 * 2 * 8;
constexpr unsigned SPIN_CAP = 1u << 22;

__device__ __forceinline__ float asf(unsigned u) { return __builtin_bit_cast(float, u); }
__device__ __forceinline__ float bf_lo(unsigned u) { return __builtin_bit_cast(float, u << 16); }
__device__ __forceinline__ float bf_hi(unsigned u) { return __builtin_bit_cast(float, u & 0xffff0000u); }
__device__ __forceinline__ unsigned f2bf(float f) {
    unsigned u = __builtin_bit_cast(unsigned, f);
    return (u + 0x7fffu + ((u >> 16) & 1u)) >> 16;
}
__device__ __forceinline__ float rbf(float f) { return __builtin_bit_cast(float, f2bf(f) << 16); }
template <int CTRL> __device__ __forceinline__ float dpp(float x) {
    return __builtin_bit_cast(float, __builtin_amdgcn_mov_dpp(__builtin_bit_cast(int, x), CTRL, 0xf, 0xf, true));
}
constexpr int XOR1 = 0xB1, XOR2 = 0x4E, XOR7 = 0x141, ROR4 = 0x124, ROR8 = 0x128;
__device__ __forceinline__ float swap32_add(float a, float b) {
    auto s = __builtin_amdgcn_permlane32_swap(__builtin_bit_cast(unsigned, a), __builtin_bit_cast(unsigned, b), false, false);
    return __builtin_bit_cast(float, (unsigned)s[0]) + __builtin_bit_cast(float, (unsigned)s[1]);
}
__device__ __forceinline__ float swap16_add(float a, float b) {
    auto s = __builtin_amdgcn_permlane16_swap(__builtin_bit_cast(unsigned, a), __builtin_bit_cast(unsigned, b), false, false);
    return __builtin_bit_cast(float, (unsigned)s[0]) + __builtin_bit_cast(float, (unsigned)s[1]);
}
__device__ __forceinline__ float swap32_max(float a) {
    auto s = __builtin_amdgcn_permlane32_swap(__builtin_bit_cast(unsigned, a), __builtin_bit_cast(unsigned, a), false, false);
    return fmaxf(__builtin_bit_cast(float, (unsigned)s[0]), __builtin_bit_cast(float, (unsigned)s[1]));
}
__device__ __forceinline__ float swap16_max(float a) {
    auto s = __builtin_amdgcn_permlane16_swap(__builtin_bit_cast(unsigned, a), __builtin_bit_cast(unsigned, a), false, false);
    return fmaxf(__builtin_bit_cast(float, (unsigned)s[0]), __builtin_bit_cast(float, (unsigned)s[1]));
}
__device__ __forceinline__ float wave_sum(float v) {
    v += dpp<XOR1>(v); v += dpp<XOR2>(v); v += dpp<ROR4>(v); v += dpp<ROR8>(v);
    v = swap16_add(v, v); v = swap32_add(v, v);
    return v;
}
__device__ __forceinline__ float wave_max(float v) {
    v = fmaxf(v, dpp<XOR1>(v)); v = fmaxf(v, dpp<XOR2>(v)); v = fmaxf(v, dpp<ROR4>(v)); v = fmaxf(v, dpp<ROR8>(v));
    v = swap16_max(v); v = swap32_max(v);
    return v;
}
__device__ __forceinline__ float dot2(unsigned a, unsigned b, float acc) {
    return __builtin_amdgcn_fdot2_f32_bf16(__builtin_bit_cast(bf16x2, a), __builtin_bit_cast(bf16x2, b), acc, false);
}
#define BAR() asm volatile("s_waitcnt lgkmcnt(0)\n\ts_barrier" ::: "memory")
#define SB() do { asm volatile("" ::: "memory"); __builtin_amdgcn_sched_barrier(0); } while (0)
#define LDG(p) __builtin_nontemporal_load((const v4u*)(p))

__device__ __forceinline__ void st_gran(unsigned char* ws, unsigned off, unsigned tag, unsigned val) {
    __hip_atomic_store((gu64*)(ws + off), ((unsigned long long)tag << 32) | val, RLX_AGENT);
}

__device__ __forceinline__ void ln_stats(const float (&v)[8], float& m, float& sd) {
    float s = ((v[0] + v[1]) + (v[2] + v[3])) + ((v[4] + v[5]) + (v[6] + v[7]));
    s = wave_sum(s);
    const float mean = s * (1.f / 512.f);
    float q = 0.f;
#pragma unroll
    for (int i = 0; i < 8; ++i) { const float d = v[i] - mean; q = fmaf(d, d, q); }
    q = wave_sum(q);
    m = rbf(mean);
    sd = rbf(sqrtf(rbf(rbf(q * (1.f / 512.f)) + 1e-5f)));
}

struct Lds {
    float big[8 * 512];
    float hn[512];
    unsigned qs[256];
    float pb[8 * 64];
    float ml[4 * 8 * 2];
    float red1[8 * 16];
    float red4[8 * 8];
    float o16[16];
};

__global__ void __launch_bounds__(NTHR) fused_block(const u16* __restrict__ x, const u16* __restrict__ kc, const u16* __restrict__ vc,
                                                     const u16* __restrict__ wq, const u16* __restrict__ wo, const u16* __restrict__ w1,
                                                     const u16* __restrict__ b1, const u16* __restrict__ w2, const u16* __restrict__ b2,
                                                     u16* __restrict__ out, unsigned char* ws) {
    __shared__ __attribute__((aligned(16))) Lds L;
    const int tid = threadIdx.x, lane = tid & 63, wave = __builtin_amdgcn_readfirstlane(tid >> 6);
    const int b = blockIdx.x, g = b & 7, s = b >> 3;
    const bool mlp = s < 28;
    const int j = g * 28 + s;
    if (wave < 8) {
        const int r = lane >> 4, c = lane & 15;
        const int r0 = tid >> 1, hf = tid & 1;
        const u16* wqp = wq + (size_t)r0 * 4096 + 512 * g + 16 * s + 8 * hf;
        const size_t kvoff = ((size_t)g * SK + (SK - WIN) + 128 * s + 16 * wave + r) * 128 + 8 * c;
        const u16* wop = wo + (size_t)(512 * g + 16 * s + 2 * wave) * 512 + 8 * lane;
        const int jc = mlp ? j : 0;
        const u16* w1p = w1 + (size_t)tid * FF + 8 * jc;
        const u16* w2p = w2 + (size_t)(8 * jc + wave) * 512 + 8 * lane;
        const u16* b1p = b1 + 8 * jc + wave;
        SB();
        const v4u xv = *(const v4u*)(x + 8 * lane);
        const unsigned xr0 = x[r0], xr1 = x[r0 + 256];
        SB();
        const v4u q0 = LDG(wqp), q1 = LDG(wqp + (size_t)256 * 4096);
        SB();
        v4u K[4], V[4];
#pragma unroll
        for (int i = 0; i < 4; ++i) K[i] = LDG(kc + kvoff + i * 512);
        SB();
#pragma unroll
        for (int i = 0; i < 4; ++i) V[i] = LDG(vc + kvoff + i * 512);
        SB();
        const unsigned epoch = __hip_atomic_load((gu32*)(ws + WS_EPOCH), RLX_AGENT);
        SB();
        const v4u o0 = LDG(wop), o1 = LDG(wop + 512);
        SB();
        const v4u w1v = LDG(w1p);
        const v4u w2v = LDG(w2p);
        const unsigned b1v = *b1p;
        SB();
        {
            float xf[8] = {bf_lo(xv.x), bf_hi(xv.x), bf_lo(xv.y), bf_hi(xv.y), bf_lo(xv.z), bf_hi(xv.z), bf_lo(xv.w), bf_hi(xv.w)};
            float m, sd;
            ln_stats(xf, m, sd);
            const float n0 = rbf(rbf(bf_lo(xr0) - m) / sd), n1 = rbf(rbf(bf_lo(xr1) - m) / sd);
            float a[8];
            a[0] = fmaf(n1, bf_lo(q1.x), n0 * bf_lo(q0.x)); a[1] = fmaf(n1, bf_hi(q1.x), n0 * bf_hi(q0.x));
            a[2] = fmaf(n1, bf_lo(q1.y), n0 * bf_lo(q0.y)); a[3] = fmaf(n1, bf_hi(q1.y), n0 * bf_hi(q0.y));
            a[4] = fmaf(n1, bf_lo(q1.z), n0 * bf_lo(q0.z)); a[5] = fmaf(n1, bf_hi(q1.z), n0 * bf_hi(q0.z));
            a[6] = fmaf(n1, bf_lo(q1.w), n0 * bf_lo(q0.w)); a[7] = fmaf(n1, bf_hi(q1.w), n0 * bf_hi(q0.w));
            float u[4];
#pragma unroll
            for (int k = 0; k < 4; ++k) u[k] = swap32_add(a[k], a[k + 4]);
            const float z0 = swap16_add(u[0], u[2]), z1 = swap16_add(u[1], u[3]);
            const float t0 = z0 + dpp<XOR2>(z0), t1 = z1 + dpp<XOR2>(z1);
            float rr = (lane & 2) ? t1 : t0;
            rr += dpp<ROR4>(rr); rr += dpp<ROR8>(rr);
            if ((lane & 12) == 0) L.red1[wave * 16 + 8 * (lane & 1) + ((lane >> 1) & 1) + 2 * ((lane >> 4) & 1) + 4 * (lane >> 5)] = rr;
        }
        BAR();
        BAR();
        {
            v4u qp[4];
#pragma unroll
            for (int h = 0; h < 4; ++h) qp[h] = *(const v4u*)&L.qs[h * 64 + 4 * c];
            float xs[16];
#pragma unroll
            for (int i = 0; i < 4; ++i)
#pragma unroll
                for (int h = 0; h < 4; ++h) {
                    float d = dot2(K[i].x, qp[h].x, 0.f);
                    d = dot2(K[i].y, qp[h].y, d); d = dot2(K[i].z, qp[h].z, d); d = dot2(K[i].w, qp[h].w, d);
                    xs[i * 4 + h] = d;
                }
#define TR_STEP(HALF, CTRL, BIT) _Pragma("unroll") for (int h = 0; h < (HALF); ++h) { \
            const float lo_ = xs[h], hi_ = xs[h + (HALF)]; const float t1_ = lo_ + dpp<CTRL>(lo_), t2_ = hi_ + dpp<CTRL>(hi_); \
            xs[h] = (c & (BIT)) ? t2_ : t1_; }
            TR_STEP(8, ROR8, 8) TR_STEP(4, XOR7, 4) TR_STEP(2, XOR2, 2) TR_STEP(1, XOR1, 1)
#undef TR_STEP
            const float sc = rbf(rbf(xs[0]) * 0.08838834764831845f);
            float m = fmaxf(sc, dpp<ROR4>(sc)); m = fmaxf(m, dpp<ROR8>(m)); m = swap16_max(m); m = swap32_max(m);
            const float p = __expf(sc - m);
            float l = p + dpp<ROR4>(p); l += dpp<ROR8>(l); l = swap16_add(l, l); l = swap32_add(l, l);
            L.pb[wave * 64 + lane] = p;
            if (lane < 4) { L.ml[(lane * 8 + wave) * 2] = m; L.ml[(lane * 8 + wave) * 2 + 1] = l; }
            asm volatile("s_waitcnt lgkmcnt(0)" ::: "memory");
            v4f pr[4];
#pragma unroll
            for (int i = 0; i < 4; ++i) pr[i] = *(const v4f*)&L.pb[wave * 64 + r * 16 + 4 * i];
            float acc[4][8];
#pragma unroll
            for (int h = 0; h < 4; ++h)
#pragma unroll
                for (int k = 0; k < 8; ++k) acc[h][k] = 0.f;
#pragma unroll
            for (int i = 0; i < 4; ++i) {
                const float vf[8] = {bf_lo(V[i].x), bf_hi(V[i].x), bf_lo(V[i].y), bf_hi(V[i].y), bf_lo(V[i].z), bf_hi(V[i].z), bf_lo(V[i].w), bf_hi(V[i].w)};
#pragma unroll
                for (int h = 0; h < 4; ++h)
#pragma unroll
                    for (int k = 0; k < 8; ++k) acc[h][k] = fmaf(pr[i][h], vf[k], acc[h][k]);
            }
            float fo[8];
#pragma unroll
            for (int k = 0; k < 8; ++k) {
                const float e0 = swap32_add(acc[0][k], acc[2][k]), e1 = swap32_add(acc[1][k], acc[3][k]);
                fo[k] = swap16_add(e0, e1);
            }
            float* dst = &L.big[wave * 512 + r * 128 + 8 * c];
            *(v4f*)dst = v4f{fo[0], fo[1], fo[2], fo[3]};
            *(v4f*)(dst + 4) = v4f{fo[4], fo[5], fo[6], fo[7]};
        }
        BAR();
        const unsigned tag = epoch + 1u;
        {
            const int h = tid >> 7;
            const v4f ma = *(const v4f*)&L.ml[h * 16], mb = *(const v4f*)&L.ml[h * 16 + 4], mc = *(const v4f*)&L.ml[h * 16 + 8], md = *(const v4f*)&L.ml[h * 16 + 12];
            const float mw[8] = {ma.x, ma.z, mb.x, mb.z, mc.x, mc.z, md.x, md.z};
            const float lw[8] = {ma.y, ma.w, mb.y, mb.w, mc.y, mc.w, md.y, md.w};
            float M = mw[0];
#pragma unroll
            for (int w = 1; w < 8; ++w) M = fmaxf(M, mw[w]);
            float o = 0.f, Ls = 0.f;
#pragma unroll
            for (int w = 0; w < 8; ++w) { const float e = __expf(mw[w] - M); o = fmaf(e, L.big[w * 512 + tid], o); Ls = fmaf(e, lw[w], Ls); }
            st_gran(ws, WS_E2O + (((g * 32 + (tid >> 4)) * 32 + s) * 16 + (tid & 15)) * 8, tag, __builtin_bit_cast(unsigned, o));
            if ((tid & 127) < 2) st_gran(ws, WS_E2ML + (((g * 4 + h) * 32 + s) * 2 + (tid & 1)) * 8, tag, __builtin_bit_cast(unsigned, (tid & 1) ? Ls : M));
        }
        BAR();
        {
            const float oa = L.o16[2 * wave], ob = L.o16[2 * wave + 1];
            float* dst = &L.big[wave * 512 + 8 * lane];
            *(v4f*)dst = v4f{fmaf(ob, bf_lo(o1.x), oa * bf_lo(o0.x)), fmaf(ob, bf_hi(o1.x), oa * bf_hi(o0.x)), fmaf(ob, bf_lo(o1.y), oa * bf_lo(o0.y)), fmaf(ob, bf_hi(o1.y), oa * bf_hi(o0.y))};
            *(v4f*)(dst + 4) = v4f{fmaf(ob, bf_lo(o1.z), oa * bf_lo(o0.z)), fmaf(ob, bf_hi(o1.z), oa * bf_hi(o0.z)), fmaf(ob, bf_lo(o1.w), oa * bf_lo(o0.w)), fmaf(ob, bf_hi(o1.w), oa * bf_hi(o0.w))};
        }
        BAR();
        {
            float a = 0.f;
#pragma unroll
            for (int w = 0; w < 8; ++w) a += L.big[w * 512 + tid];
            st_gran(ws, WS_E3A + (((tid >> 1) * 256 + b) * 2 + (tid & 1)) * 8, tag, __builtin_bit_cast(unsigned, a));
        }
        if (!mlp) return;
        BAR();
        {
            const float hv = L.hn[tid];
            float a[8] = {hv * bf_lo(w1v.x), hv * bf_hi(w1v.x), hv * bf_lo(w1v.y), hv * bf_hi(w1v.y), hv * bf_lo(w1v.z), hv * bf_hi(w1v.z), hv * bf_lo(w1v.w), hv * bf_hi(w1v.w)};
            float u[4];
#pragma unroll
            for (int k = 0; k < 4; ++k) u[k] = swap32_add(a[k], a[k + 4]);
            const float z0 = swap16_add(u[0], u[2]), z1 = swap16_add(u[1], u[3]);
            const float t0 = z0 + dpp<XOR2>(z0), t1 = z1 + dpp<XOR2>(z1);
            float rr = (lane & 2) ? t1 : t0;
            rr += dpp<XOR1>(rr); rr += dpp<ROR4>(rr); rr += dpp<ROR8>(rr);
            if ((lane & 13) == 0) L.red4[(((lane >> 1) & 1) + 2 * ((lane >> 4) & 1) + 4 * (lane >> 5)) * 8 + wave] = rr;
        }
        BAR();
        {
            const v4f ra = *(const v4f*)&L.red4[wave * 8], rb = *(const v4f*)&L.red4[wave * 8 + 4];
            const float sum = ((ra.x + ra.y) + (ra.z + ra.w)) + ((rb.x + rb.y) + (rb.z + rb.w));
            const float u = rbf(rbf(sum) + bf_lo(b1v));
            const float z = 0.7978845608028654f * (u + 0.044715f * u * u * u);
            const float th = 1.f - 2.f / (__expf(2.f * z) + 1.f);
            const float gl = rbf(0.5f * u * (1.f + th));
            float* dst = &L.big[wave * 512 + 8 * lane];
            *(v4f*)dst = v4f{gl * bf_lo(w2v.x), gl * bf_hi(w2v.x), gl * bf_lo(w2v.y), gl * bf_hi(w2v.y)};
            *(v4f*)(dst + 4) = v4f{gl * bf_lo(w2v.z), gl * bf_hi(w2v.z), gl * bf_lo(w2v.w), gl * bf_hi(w2v.w)};
        }
        BAR();
        {
            float a = 0.f;
#pragma unroll
            for (int w = 0; w < 8; ++w) a += L.big[w * 512 + tid];
            st_gran(ws, WS_E5 + (((tid >> 1) * 256 + j) * 2 + (tid & 1)) * 8, tag, __builtin_bit_cast(unsigned, a));
        }
        return;
    }
    {
        const __amdgpu_buffer_rsrc_t rs = __builtin_amdgcn_make_buffer_rsrc(ws, 0, WS_END, 0x00020000);
        const unsigned tag = __hip_atomic_load((gu32*)(ws + WS_EPOCH), RLX_AGENT) + 1u;
        const unsigned xpair = *(const unsigned*)(x + 2 * b);
        const unsigned b2pair = *(const unsigned*)(b2 + 2 * b);
        BAR();
        if (lane < 8) {
            float a0 = 0.f, a1 = 0.f;
#pragma unroll
            for (int w = 0; w < 8; ++w) { const v2f t = *(const v2f*)&L.red1[w * 16 + 2 * lane]; a0 += t.x; a1 += t.y; }
            st_gran(ws, WS_E1 + (g * 256 + s * 8 + lane) * 8, tag, f2bf(a0) | (f2bf(a1) << 16));
        }
        {
            v4u v[2];
            for (unsigned spin = 0;; ++spin) {
                bool ok = true;
#pragma unroll
                for (int k = 0; k < 2; ++k) { v[k] = __builtin_amdgcn_raw_buffer_load_b128(rs, WS_E1 + g * 2048 + k * 1024 + lane * 16, 0, 16); ok &= (v[k].y == tag) & (v[k].w == tag); }
                if (__all(ok) || spin > SPIN_CAP) break;
                __builtin_amdgcn_s_sleep(1);
                asm volatile("" ::: "memory");
            }
            *(v2u*)&L.qs[2 * lane] = v2u{v[0].x, v[0].z};
            *(v2u*)&L.qs[128 + 2 * lane] = v2u{v[1].x, v[1].z};
        }
        BAR();
        BAR();
        {
            v4u v[4], mlv;
            for (unsigned spin = 0;; ++spin) {
                bool ok = true;
#pragma unroll
                for (int k = 0; k < 4; ++k) { v[k] = __builtin_amdgcn_raw_buffer_load_b128(rs, WS_E2O + (g * 32 + s) * 4096 + k * 1024 + lane * 16, 0, 16); ok &= (v[k].y == tag) & (v[k].w == tag); }
                mlv = __builtin_amdgcn_raw_buffer_load_b128(rs, WS_E2ML + (g * 4 + (s >> 3)) * 512 + (lane & 31) * 16, 0, 16);
                ok &= (mlv.y == tag) & (mlv.w == tag);
                if (__all(ok) || spin > SPIN_CAP) break;
                __builtin_amdgcn_s_sleep(1);
                asm volatile("" ::: "memory");
            }
            const float ms = asf(mlv.x), ls = asf(mlv.z);
            const float M = wave_max(ms);
            const float wgt = __expf(ms - M);
            const float Lt = 0.5f * wave_sum(wgt * ls);
            float a0 = 0.f, a1 = 0.f;
#pragma unroll
            for (int k = 0; k < 4; ++k) {
                const float wk = __shfl(wgt, (lane >> 3) + 8 * k);
                a0 = fmaf(wk, asf(v[k].x), a0); a1 = fmaf(wk, asf(v[k].z), a1);
            }
            a0 += dpp<ROR8>(a0); a1 += dpp<ROR8>(a1);
            a0 = swap16_add(a0, a0); a1 = swap16_add(a1, a1);
            a0 = swap32_add(a0, a0); a1 = swap32_add(a1, a1);
            if (lane < 8) *(v2f*)&L.o16[2 * lane] = v2f{rbf(a0 / Lt), rbf(a1 / Lt)};
        }
        BAR();
        BAR();
        float h0, h1;
        {
            v4u v[4];
            for (unsigned spin = 0;; ++spin) {
                bool ok = true;
#pragma unroll
                for (int k = 0; k < 4; ++k) { v[k] = __builtin_amdgcn_raw_buffer_load_b128(rs, WS_E3A + b * 4096 + k * 1024 + lane * 16, 0, 16); ok &= (v[k].y == tag) & (v[k].w == tag); }
                if (__all(ok) || spin > SPIN_CAP) break;
                __builtin_amdgcn_s_sleep(1);
                asm volatile("" ::: "memory");
            }
            float a0 = (asf(v[0].x) + asf(v[1].x)) + (asf(v[2].x) + asf(v[3].x));
            float a1 = (asf(v[0].z) + asf(v[1].z)) + (asf(v[2].z) + asf(v[3].z));
            a0 = wave_sum(a0); a1 = wave_sum(a1);
            h0 = rbf(bf_lo(xpair) + rbf(a0)); h1 = rbf(bf_hi(xpair) + rbf(a1));
            if (lane < 8) st_gran(ws, WS_E3B + (lane * 256 + b) * 8, tag, f2bf(h0) | (f2bf(h1) << 16));
        }
        if (mlp) {
            v4u v[2];
            for (unsigned spin = 0;; ++spin) {
                bool ok = true;
#pragma unroll
                for (int k = 0; k < 2; ++k) { v[k] = __builtin_amdgcn_raw_buffer_load_b128(rs, WS_E3B + g * 2048 + k * 1024 + lane * 16, 0, 16); ok &= (v[k].y == tag) & (v[k].w == tag); }
                if (__all(ok) || spin > SPIN_CAP) break;
                __builtin_amdgcn_s_sleep(1);
                asm volatile("" ::: "memory");
            }
            float hf[8] = {bf_lo(v[0].x), bf_hi(v[0].x), bf_lo(v[0].z), bf_hi(v[0].z), bf_lo(v[1].x), bf_hi(v[1].x), bf_lo(v[1].z), bf_hi(v[1].z)};
            float m, sd;
            ln_stats(hf, m, sd);
            *(v4f*)&L.hn[4 * lane] = v4f{rbf(rbf(hf[0] - m) / sd), rbf(rbf(hf[1] - m) / sd), rbf(rbf(hf[2] - m) / sd), rbf(rbf(hf[3] - m) / sd)};
            *(v4f*)&L.hn[256 + 4 * lane] = v4f{rbf(rbf(hf[4] - m) / sd), rbf(rbf(hf[5] - m) / sd), rbf(rbf(hf[6] - m) / sd), rbf(rbf(hf[7] - m) / sd)};
            BAR();
            BAR();
            BAR();
        }
        {
            v4u v[4];
            for (unsigned spin = 0;; ++spin) {
                bool ok = true;
#pragma unroll
                for (int k = 0; k < 4; ++k) {
                    const int pidx = (k < 3 || lane < 32) ? k * 64 + lane : lane;
                    v[k] = __builtin_amdgcn_raw_buffer_load_b128(rs, WS_E5 + b * 4096 + pidx * 16, 0, 16); ok &= (v[k].y == tag) & (v[k].w == tag);
                }
                if (__all(ok) || spin > SPIN_CAP) break;
                __builtin_amdgcn_s_sleep(1);
                asm volatile("" ::: "memory");
            }
            const float l0 = lane < 32 ? asf(v[3].x) : 0.f, l1 = lane < 32 ? asf(v[3].z) : 0.f;
            float a0 = (asf(v[0].x) + asf(v[1].x)) + (asf(v[2].x) + l0);
            float a1 = (asf(v[0].z) + asf(v[1].z)) + (asf(v[2].z) + l1);
            a0 = wave_sum(a0); a1 = wave_sum(a1);
            const float y0 = rbf(rbf(a0) + bf_lo(b2pair)), y1 = rbf(rbf(a1) + bf_hi(b2pair));
            if (lane == 0) {
                *(unsigned*)(out + 2 * b) = f2bf(h0 + y0) | (f2bf(h1 + y1) << 16);
                if (b == 0) __hip_atomic_store((gu32*)(ws + WS_EPOCH), tag, RLX_AGENT);
            }
        }
    }
}

extern "C" void kernel_launch(void* const* d_in, const int* in_sizes, int n_in, void* d_out, int out_size, void* d_ws, size_t ws_size, hipStream_t stream) {
    (void)in_sizes; (void)n_in; (void)out_size; (void)ws_size;
    fused_block<<<NWG, NTHR, 0, stream>>>((const u16*)d_in[0], (const u16*)d_in[1], (const u16*)d_in[2], (const u16*)d_in[3], (const u16*)d_in[4],
                                          (const u16*)d_in[6], (const u16*)d_in[7], (const u16*)d_in[8], (const u16*)d_in[9], (u16*)d_out, (unsigned char*)d_ws);
}
```

```cpp
#include <hip/hip_runtime.h>
#include <stdint.h>
#define ORDER 0

typedef unsigned short u16;
typedef unsigned v4u __attribute__((ext_vector_type(4)));
typedef unsigned v2u __attribute__((ext_vector_type(2)));
typedef float v4f __attribute__((ext_vector_type(4)));
typedef float v2f __attribute__((ext_vector_type(2)));
typedef __bf16 bf16x2 __attribute__((ext_vector_type(2)));
#define GAS __attribute__((address_space(1)))
typedef GAS unsigned long long gu64;
typedef GAS unsigned gu32;
#define RLX_AGENT __ATOMIC_RELAXED, __HIP_MEMORY_SCOPE_AGENT

constexpr int SK = 65536, WIN = 4096, FF = 1792;
constexpr int NWG = 256, NTHR = 576;
constexpr unsigned WS_EPOCH = 0;
constexpr unsigned WS_E1 = 4096;
constexpr unsigned WS_E2O = WS_E1 + 8 * 256 * 8;
constexpr unsigned WS_E2ML = WS_E2O + 8 * 8 * 8 * 32 * 8 * 8;
constexpr unsigned WS_E3A = WS_E2ML + 8 * 4 * 32 * 2 * 8;
constexpr unsigned WS_E3B = WS_E3A + 64 * 64 * 8 * 8;
constexpr unsigned WS_E5 = WS_E3B + 8 * 256 * 8;
constexpr unsigned WS_END = WS_E5 + 64 * 64 * 8 * 8;
constexpr unsigned SPIN_CAP = 1u << 22;

__device__ __forceinline__ float asf(unsigned u) { return __builtin_bit_cast(float, u); }
__device__ __forceinline__ unsigned asu(float f) { return __builtin_bit_cast(unsigned, f); }
__device__ __forceinline__ float bf_lo(unsigned u) { return __builtin_bit_cast(float, u << 16); }
__device__ __forceinline__ float bf_hi(unsigned u) { return __builtin_bit_cast(float, u & 0xffff0000u); }
__device__ __forceinline__ unsigned f2bf(float f) {
    unsigned u = __builtin_bit_cast(unsigned, f);
    return (u + 0x7fffu + ((u >> 16) & 1u)) >> 16;
}
__device__ __forceinline__ float rbf(float f) { return __builtin_bit_cast(float, f2bf(f) << 16); }
template <int CTRL> __device__ __forceinline__ float dpp(float x) {
    return __builtin_bit_cast(float, __builtin_amdgcn_mov_dpp(__builtin_bit_cast(int, x), CTRL, 0xf, 0xf, true));
}
constexpr int XOR1 = 0xB1, XOR2 = 0x4E, XOR7 = 0x141, ROR4 = 0x124, ROR8 = 0x128;
__device__ __forceinline__ float swap32_add(float a, float b) {
    auto s = __builtin_amdgcn_permlane32_swap(asu(a), asu(b), false, false);
    return asf(s[0]) + asf(s[1]);
}
__device__ __forceinline__ float swap16_add(float a, float b) {
    auto s = __builtin_amdgcn_permlane16_swap(asu(a), asu(b), false, false);
    return asf(s[0]) + asf(s[1]);
}
__device__ __forceinline__ float swap32_max(float a) {
    auto s = __builtin_amdgcn_permlane32_swap(asu(a), asu(a), false, false);
    return fmaxf(asf(s[0]), asf(s[1]));
}
__device__ __forceinline__ float swap16_max(float a) {
    auto s = __builtin_amdgcn_permlane16_swap(asu(a), asu(a), false, false);
    return fmaxf(asf(s[0]), asf(s[1]));
}
__device__ __forceinline__ float wave_sum(float v) {
    v += dpp<XOR1>(v); v += dpp<XOR2>(v); v += dpp<ROR4>(v); v += dpp<ROR8>(v);
    v = swap16_add(v, v); v = swap32_add(v, v);
    return v;
}
__device__ __forceinline__ float wave_max(float v) {
    v = fmaxf(v, dpp<XOR1>(v)); v = fmaxf(v, dpp<XOR2>(v)); v = fmaxf(v, dpp<ROR4>(v)); v = fmaxf(v, dpp<ROR8>(v));
    v = swap16_max(v); v = swap32_max(v);
    return v;
}
__device__ __forceinline__ float sum_hi4(float v) {
    v += dpp<ROR4>(v); v += dpp<ROR8>(v);
    v = swap16_add(v, v); v = swap32_add(v, v);
    return v;
}
__device__ __forceinline__ float dot2(unsigned a, unsigned b, float acc) {
    return __builtin_amdgcn_fdot2_f32_bf16(__builtin_bit_cast(bf16x2, a), __builtin_bit_cast(bf16x2, b), acc, false);
}
__device__ __forceinline__ float rdl(float v, int l) { return asf(__builtin_amdgcn_readlane(asu(v), l)); }
#define BAR() asm volatile("s_waitcnt lgkmcnt(0)\n\ts_barrier" ::: "memory")
#define STAMP(i) do { } while (0)
#define SB() do { asm volatile("" ::: "memory"); __builtin_amdgcn_sched_barrier(0); } while (0)
#define LDG(p) __builtin_nontemporal_load((const v4u*)(p))
#define LD16(off) __builtin_amdgcn_raw_buffer_load_b128(rs, (off), 0, 16)
#define ST16(off, a, b) __builtin_amdgcn_raw_buffer_store_b128(v4u{(a), tag, (b), tag}, rs, (off), 0, 16)
#define TAGOK(v) (((v).y == tag) & ((v).w == tag))
#define SPIN_END() if (__all(ok) || spin > SPIN_CAP) break; asm volatile("" ::: "memory")

__device__ __forceinline__ void st_gran(unsigned char* ws, unsigned off, unsigned tag, unsigned val) {
    __hip_atomic_store((gu64*)(ws + off), ((unsigned long long)tag << 32) | val, RLX_AGENT);
}

__device__ __forceinline__ void ln_stats(const float (&v)[8], float& m, float& sd) {
    float s = ((v[0] + v[1]) + (v[2] + v[3])) + ((v[4] + v[5]) + (v[6] + v[7]));
    s = wave_sum(s);
    const float mean = s * (1.f / 512.f);
    float q = 0.f;
#pragma unroll
    for (int i = 0; i < 8; ++i) { const float d = v[i] - mean; q = fmaf(d, d, q); }
    q = wave_sum(q);
    m = rbf(mean);
    sd = rbf(sqrtf(rbf(rbf(q * (1.f / 512.f)) + 1e-5f)));
}
__device__ __forceinline__ void fma8(float (&a)[8], float sc, const v4u& w) {
    a[0] = fmaf(sc, bf_lo(w.x), a[0]); a[1] = fmaf(sc, bf_hi(w.x), a[1]); a[2] = fmaf(sc, bf_lo(w.y), a[2]); a[3] = fmaf(sc, bf_hi(w.y), a[3]);
    a[4] = fmaf(sc, bf_lo(w.z), a[4]); a[5] = fmaf(sc, bf_hi(w.z), a[5]); a[6] = fmaf(sc, bf_lo(w.w), a[6]); a[7] = fmaf(sc, bf_hi(w.w), a[7]);
}

struct Lds {
    float big[8 * 512];
    unsigned qs[256];
    float pb[8 * 64];
    float ml[4 * 8 * 2];
    float red1[8 * 16];
    float red4[32 * 8];
};

__global__ void __launch_bounds__(NTHR) fused_block(const u16* __restrict__ x, const u16* __restrict__ kc, const u16* __restrict__ vc,
                                                     const u16* __restrict__ wq, const u16* __restrict__ wo, const u16* __restrict__ w1,
                                                     const u16* __restrict__ b1, const u16* __restrict__ w2, const u16* __restrict__ b2,
                                                     u16* __restrict__ out, unsigned char* ws) {
    __shared__ __attribute__((aligned(16))) Lds L;
    const int tid = threadIdx.x, lane = tid & 63, wave = __builtin_amdgcn_readfirstlane(tid >> 6);
    const int b = blockIdx.x, g = b & 7, s = b >> 3;
    const bool is_wo = s < 8, is_mlp = (s >= 8) & (s < 15), is_red = (s >= 16) & (s < 24);
    const __amdgpu_buffer_rsrc_t rs = __builtin_amdgcn_make_buffer_rsrc(ws, 0, WS_END, 0x00020000);
    STAMP(0);

    if (wave < 8) {
        const int r = lane >> 4, c = lane & 15;
        const int r0 = tid >> 1, hf = tid & 1;
        const u16* wqp = wq + (size_t)r0 * 4096 + 512 * g + 16 * s + 8 * hf;
        const size_t kvoff = ((size_t)g * SK + (SK - WIN) + 128 * s + 16 * wave + r) * 128 + 8 * c;
        const int j = g * 7 + (s - 8);
        const u16* rp0; const u16* rp1; size_t st0, st1;
        if (is_mlp) { rp0 = w1 + (size_t)(64 * wave + c) * FF + 32 * j + 8 * r; st0 = (size_t)16 * FF; rp1 = w2 + (size_t)(32 * j + 4 * wave) * 512 + 8 * lane; st1 = 512; }
        else if (is_wo) { rp0 = wo + (size_t)(512 * g + 64 * s + 8 * wave) * 512 + 8 * lane; st0 = 512; rp1 = rp0 + 4 * 512; st1 = 512; }
        else { rp0 = x + 8 * lane; st0 = 0; rp1 = rp0; st1 = 0; }
        const u16* b1p = is_mlp ? b1 + 32 * j + 4 * wave + 2 * (lane & 1) : x;
        SB();
        const v4u xv = *(const v4u*)(x + 8 * lane);
        const unsigned xr0 = x[r0], xr1 = x[r0 + 256];
        const unsigned epoch = __hip_atomic_load((gu32*)(ws + WS_EPOCH), RLX_AGENT);
        SB();
        const v4u q0 = LDG(wqp), q1 = LDG(wqp + (size_t)256 * 4096);
        SB();
        v4u K[4], V[4], R[8];
#pragma unroll
        for (int i = 0; i < 4; ++i) K[i] = LDG(kc + kvoff + i * 512);
        SB();
        {
            float xf[8] = {bf_lo(xv.x), bf_hi(xv.x), bf_lo(xv.y), bf_hi(xv.y), bf_lo(xv.z), bf_hi(xv.z), bf_lo(xv.w), bf_hi(xv.w)};
            float m, sd;
            ln_stats(xf, m, sd);
            const float n0 = rbf(rbf(bf_lo(xr0) - m) / sd), n1 = rbf(rbf(bf_lo(xr1) - m) / sd);
            float a[8] = {0.f, 0.f, 0.f, 0.f, 0.f, 0.f, 0.f, 0.f};
            fma8(a, n0, q0); fma8(a, n1, q1);
            float u[4];
#pragma unroll
            for (int k = 0; k < 4; ++k) u[k] = swap32_add(a[k], a[k + 4]);
            const float z0 = swap16_add(u[0], u[2]), z1 = swap16_add(u[1], u[3]);
            const float t0 = z0 + dpp<XOR2>(z0), t1 = z1 + dpp<XOR2>(z1);
            float rr = (lane & 2) ? t1 : t0;
            rr += dpp<ROR4>(rr); rr += dpp<ROR8>(rr);
            if ((lane & 12) == 0) L.red1[wave * 16 + 8 * (lane & 1) + ((lane >> 1) & 1) + 2 * ((lane >> 4) & 1) + 4 * (lane >> 5)] = rr;
        }
        SB();
        STAMP(11);
        BAR();
        BAR();
#pragma unroll
        for (int i = 0; i < 4; ++i) V[i] = LDG(vc + kvoff + i * 512);
        SB();
        BAR();
        {
            v4u qp[4];
#pragma unroll
            for (int h = 0; h < 4; ++h) qp[h] = *(const v4u*)&L.qs[h * 64 + 4 * c];
            float xs[16];
#pragma unroll
            for (int i = 0; i < 4; ++i)
#pragma unroll
                for (int h = 0; h < 4; ++h) {
                    float d = dot2(K[i].x, qp[h].x, 0.f);
                    d = dot2(K[i].y, qp[h].y, d); d = dot2(K[i].z, qp[h].z, d); d = dot2(K[i].w, qp[h].w, d);
                    xs[i * 4 + h] = d;
                }
#define TR_STEP(HALF, CTRL, BIT) _Pragma("unroll") for (int h = 0; h < (HALF); ++h) { \
            const float lo_ = xs[h], hi_ = xs[h + (HALF)]; const float t1_ = lo_ + dpp<CTRL>(lo_), t2_ = hi_ + dpp<CTRL>(hi_); \
            xs[h] = (c & (BIT)) ? t2_ : t1_; }
            TR_STEP(8, ROR8, 8) TR_STEP(4, XOR7, 4) TR_STEP(2, XOR2, 2) TR_STEP(1, XOR1, 1)
            const float sc = rbf(rbf(xs[0]) * 0.08838834764831845f);
            float m = fmaxf(sc, dpp<ROR4>(sc)); m = fmaxf(m, dpp<ROR8>(m)); m = swap16_max(m); m = swap32_max(m);
            const float p = __expf(sc - m);
            float l = p + dpp<ROR4>(p); l += dpp<ROR8>(l); l = swap16_add(l, l); l = swap32_add(l, l);
            L.pb[wave * 64 + lane] = p;
            if (lane < 4) { L.ml[(lane * 8 + wave) * 2] = m; L.ml[(lane * 8 + wave) * 2 + 1] = l; }
            asm volatile("s_waitcnt lgkmcnt(0)" ::: "memory");
            v4f pr[4];
#pragma unroll
            for (int i = 0; i < 4; ++i) pr[i] = *(const v4f*)&L.pb[wave * 64 + r * 16 + 4 * i];
            float acc[4][8];
#pragma unroll
            for (int h = 0; h < 4; ++h)
#pragma unroll
                for (int k = 0; k < 8; ++k) acc[h][k] = 0.f;
#pragma unroll
            for (int i = 0; i < 4; ++i)
#pragma unroll
                for (int h = 0; h < 4; ++h) fma8(acc[h], pr[i][h], V[i]);
            float fo[8];
#pragma unroll
            for (int k = 0; k < 8; ++k) {
                const float e0 = swap32_add(acc[0][k], acc[2][k]), e1 = swap32_add(acc[1][k], acc[3][k]);
                fo[k] = swap16_add(e0, e1);
            }
            float* dst = &L.big[wave * 512 + r * 128 + 8 * c];
            *(v4f*)dst = v4f{fo[0], fo[1], fo[2], fo[3]};
            *(v4f*)(dst + 4) = v4f{fo[4], fo[5], fo[6], fo[7]};
        }
        BAR();
        const unsigned tag = epoch + 1u;
        if (wave < 4) {
            const int h = wave, d = 2 * lane;
            const v4f ma = *(const v4f*)&L.ml[h * 16], mb = *(const v4f*)&L.ml[h * 16 + 4], mc = *(const v4f*)&L.ml[h * 16 + 8], md = *(const v4f*)&L.ml[h * 16 + 12];
            const float mw[8] = {ma.x, ma.z, mb.x, mb.z, mc.x, mc.z, md.x, md.z};
            const float lw[8] = {ma.y, ma.w, mb.y, mb.w, mc.y, mc.w, md.y, md.w};
            float M = mw[0];
#pragma unroll
            for (int w = 1; w < 8; ++w) M = fmaxf(M, mw[w]);
            float oa = 0.f, ob = 0.f, Ls = 0.f;
#pragma unroll
            for (int w = 0; w < 8; ++w) { const float e = __expf(mw[w] - M); const v2f t = *(const v2f*)&L.big[w * 512 + 2 * tid]; oa = fmaf(e, t.x, oa); ob = fmaf(e, t.y, ob); Ls = fmaf(e, lw[w], Ls); }
            ST16(WS_E2O + ((((g * 8 + 2 * h + (d >> 6)) * 8 + ((d >> 3) & 7)) * 32 + s) * 8 + (d & 7)) * 8, asu(oa), asu(ob));
            if (lane == 0) ST16(WS_E2ML + ((g * 4 + h) * 32 + s) * 16, asu(M), asu(Ls));
        }
        STAMP(3);
        if (!(is_wo | is_mlp)) {
            return;
        }
        BAR();
#pragma unroll
        for (int i = 0; i < 4; ++i) R[i] = LDG(rp0 + i * st0);
#pragma unroll
        for (int i = 0; i < 4; ++i) R[4 + i] = LDG(rp1 + i * st1);
        const unsigned b1v = *(const unsigned*)b1p;
        SB();
        if (is_wo) {
            v4u v[2], mlv;
            for (unsigned spin = 0;; ++spin) {
                bool ok = true;
#pragma unroll
                for (int k = 0; k < 2; ++k) { v[k] = LD16(WS_E2O + ((g * 8 + s) * 8 + wave) * 2048 + k * 1024 + lane * 16); ok &= TAGOK(v[k]); }
                mlv = LD16(WS_E2ML + (g * 4 + (s >> 1)) * 512 + (lane & 31) * 16);
                ok &= TAGOK(mlv);
                SPIN_END();
            }
            const float ms = asf(mlv.x), ls = asf(mlv.z);
            const float M = wave_max(ms);
            const float wgt = __expf(ms - M);
            const float Lt = 0.5f * wave_sum(wgt * ls);
            const float wa = __shfl(wgt, lane >> 2), wb = __shfl(wgt, (lane >> 2) + 16);
            float a0 = fmaf(wb, asf(v[1].x), wa * asf(v[0].x)), a1 = fmaf(wb, asf(v[1].z), wa * asf(v[0].z));
            a0 = rbf(sum_hi4(a0) / Lt); a1 = rbf(sum_hi4(a1) / Lt);
            float acc[8] = {0.f, 0.f, 0.f, 0.f, 0.f, 0.f, 0.f, 0.f};
#pragma unroll
            for (int i = 0; i < 4; ++i) { fma8(acc, rdl(a0, i), R[2 * i]); fma8(acc, rdl(a1, i), R[2 * i + 1]); }
            STAMP(4);
            float* dst = &L.big[wave * 512 + 8 * lane];
            *(v4f*)dst = v4f{acc[0], acc[1], acc[2], acc[3]};
            *(v4f*)(dst + 4) = v4f{acc[4], acc[5], acc[6], acc[7]};
            BAR();
            if (wave < 4) {
                float s0 = 0.f, s1 = 0.f;
#pragma unroll
                for (int w = 0; w < 8; ++w) { const v2f t = *(const v2f*)&L.big[w * 512 + 2 * tid]; s0 += t.x; s1 += t.y; }
                ST16(WS_E3A + (((tid >> 2) * 64 + (g * 8 + s)) * 8 + 2 * (tid & 3)) * 8, asu(s0), asu(s1));
            }
            STAMP(5);
        } else if (is_mlp) {
            v4u v[2]; v2u hr[4];
            for (unsigned spin = 0;; ++spin) {
                bool ok = true;
#pragma unroll
                for (int k = 0; k < 2; ++k) { v[k] = LD16(WS_E3B + g * 2048 + k * 1024 + lane * 16); ok &= TAGOK(v[k]); }
#pragma unroll
                for (int i = 0; i < 4; ++i) { hr[i] = __builtin_amdgcn_raw_buffer_load_b64(rs, WS_E3B + g * 2048 + ((64 * wave + 16 * i + c) >> 1) * 8, 0, 16); ok &= (hr[i].y == tag); }
                SPIN_END();
            }
            STAMP(6);
            float hf[8] = {bf_lo(v[0].x), bf_hi(v[0].x), bf_lo(v[0].z), bf_hi(v[0].z), bf_lo(v[1].x), bf_hi(v[1].x), bf_lo(v[1].z), bf_hi(v[1].z)};
            float m, sd;
            ln_stats(hf, m, sd);
            float xs[8] = {0.f, 0.f, 0.f, 0.f, 0.f, 0.f, 0.f, 0.f};
#pragma unroll
            for (int i = 0; i < 4; ++i) { const float hv = (c & 1) ? bf_hi(hr[i].x) : bf_lo(hr[i].x); fma8(xs, rbf(rbf(hv - m) / sd), R[i]); }
            TR_STEP(4, ROR8, 8) TR_STEP(2, XOR7, 4) TR_STEP(1, XOR2, 2)
            float rr = xs[0] + dpp<XOR1>(xs[0]);
            if ((c & 1) == 0) L.red4[(8 * r + ((c >> 3) & 1) * 4 + ((c >> 2) & 1) * 2 + ((c >> 1) & 1)) * 8 + wave] = rr;
            BAR();
            float acc[8] = {0.f, 0.f, 0.f, 0.f, 0.f, 0.f, 0.f, 0.f};
#pragma unroll
            for (int i = 0; i < 4; ++i) {
                const v4f ra = *(const v4f*)&L.red4[(4 * wave + i) * 8], rb = *(const v4f*)&L.red4[(4 * wave + i) * 8 + 4];
                const float sum = ((ra.x + ra.y) + (ra.z + ra.w)) + ((rb.x + rb.y) + (rb.z + rb.w));
                const unsigned bw = __builtin_amdgcn_readlane(b1v, i >> 1);
                const float u = rbf(rbf(sum) + ((i & 1) ? bf_hi(bw) : bf_lo(bw)));
                const float z = 0.7978845608028654f * (u + 0.044715f * u * u * u);
                const float th = 1.f - 2.f / (__expf(2.f * z) + 1.f);
                fma8(acc, rbf(0.5f * u * (1.f + th)), R[4 + i]);
            }
            float* dst = &L.big[wave * 512 + 8 * lane];
            *(v4f*)dst = v4f{acc[0], acc[1], acc[2], acc[3]};
            *(v4f*)(dst + 4) = v4f{acc[4], acc[5], acc[6], acc[7]};
            BAR();
            if (wave < 4) {
                float s0 = 0.f, s1 = 0.f;
#pragma unroll
                for (int w = 0; w < 8; ++w) { const v2f t = *(const v2f*)&L.big[w * 512 + 2 * tid]; s0 += t.x; s1 += t.y; }
                ST16(WS_E5 + (((tid >> 2) * 64 + j) * 8 + 2 * (tid & 3)) * 8, asu(s0), asu(s1));
            }
            STAMP(7);
        }
        return;
    }
#undef TR_STEP
    {
        const unsigned tag = __hip_atomic_load((gu32*)(ws + WS_EPOCH), RLX_AGENT) + 1u;
        const int rj = g * 8 + (s - 16);
        const int rc = is_red ? 8 * rj + 2 * (lane & 3) : 0;
        const unsigned xpair = *(const unsigned*)(x + rc);
        const unsigned b2pair = *(const unsigned*)(b2 + rc);
        BAR();
        STAMP(1);
        if (lane < 8) {
            float a0 = 0.f, a1 = 0.f;
#pragma unroll
            for (int w = 0; w < 8; ++w) { const v2f t = *(const v2f*)&L.red1[w * 16 + 2 * lane]; a0 += t.x; a1 += t.y; }
            st_gran(ws, WS_E1 + (g * 256 + s * 8 + lane) * 8, tag, f2bf(a0) | (f2bf(a1) << 16));
        }
        BAR();
        {
            v4u v[2];
            for (unsigned spin = 0;; ++spin) {
                bool ok = true;
#pragma unroll
                for (int k = 0; k < 2; ++k) { v[k] = LD16(WS_E1 + g * 2048 + k * 1024 + lane * 16); ok &= TAGOK(v[k]); }
                SPIN_END();
            }
            *(v2u*)&L.qs[2 * lane] = v2u{v[0].x, v[0].z};
            *(v2u*)&L.qs[128 + 2 * lane] = v2u{v[1].x, v[1].z};
        }
        STAMP(2);
        BAR();
        if (is_red) {
            BAR();
            float h0, h1;
            {
                v4u v[4];
                for (unsigned spin = 0;; ++spin) {
                    bool ok = true;
#pragma unroll
                    for (int k = 0; k < 4; ++k) { v[k] = LD16(WS_E3A + rj * 4096 + k * 1024 + lane * 16); ok &= TAGOK(v[k]); }
                    SPIN_END();
                }
                float a0 = (asf(v[0].x) + asf(v[1].x)) + (asf(v[2].x) + asf(v[3].x));
                float a1 = (asf(v[0].z) + asf(v[1].z)) + (asf(v[2].z) + asf(v[3].z));
                a0 = sum_hi4(a0); a1 = sum_hi4(a1);
                h0 = rbf(bf_lo(xpair) + rbf(a0)); h1 = rbf(bf_hi(xpair) + rbf(a1));
                if (lane < 32) st_gran(ws, WS_E3B + ((lane >> 2) * 256 + 4 * rj + (lane & 3)) * 8, tag, f2bf(h0) | (f2bf(h1) << 16));
            }
            STAMP(8);
            {
                v4u v[4];
                for (unsigned spin = 0;; ++spin) {
                    bool ok = true;
#pragma unroll
                    for (int k = 0; k < 4; ++k) { const int o = (k < 3 || lane < 32) ? k * 1024 + lane * 16 : lane * 16; v[k] = LD16(WS_E5 + rj * 4096 + o); ok &= TAGOK(v[k]); }
                    SPIN_END();
                }
                const float l0 = lane < 32 ? asf(v[3].x) : 0.f, l1 = lane < 32 ? asf(v[3].z) : 0.f;
                float a0 = (asf(v[0].x) + asf(v[1].x)) + (asf(v[2].x) + l0);
                float a1 = (asf(v[0].z) + asf(v[1].z)) + (asf(v[2].z) + l1);
                a0 = sum_hi4(a0); a1 = sum_hi4(a1);
                const float y0 = rbf(rbf(a0) + bf_lo(b2pair)), y1 = rbf(rbf(a1) + bf_hi(b2pair));
                if (lane < 4) *(unsigned*)(out + rc) = f2bf(h0 + y0) | (f2bf(h1 + y1) << 16);
                if (lane == 0 && rj == 0) __hip_atomic_store((gu32*)(ws + WS_EPOCH), tag, RLX_AGENT);
            }
            STAMP(9);
        }
    }
}

extern "C" void kernel_launch(void* const* d_in, const int* in_sizes, int n_in, void* d_out, int out_size, void* d_ws, size_t ws_size, hipStream_t stream) {
    (void)in_sizes; (void)n_in; (void)out_size; (void)ws_size;
    fused_block<<<NWG, NTHR, 0, stream>>>((const u16*)d_in[0], (const u16*)d_in[1], (const u16*)d_in[2], (const u16*)d_in[3], (const u16*)d_in[4],
                                          (const u16*)d_in[6], (const u16*)d_in[7], (const u16*)d_in[8], (const u16*)d_in[9], (u16*)d_out, (unsigned char*)d_ws);
}
```

```cpp
#define TOUCH
#include <hip/hip_runtime.h>
#include <stdint.h>
#define ORDER 0

typedef unsigned short u16;
typedef unsigned v4u __attribute__((ext_vector_type(4)));
typedef unsigned v2u __attribute__((ext_vector_type(2)));
typedef float v4f __attribute__((ext_vector_type(4)));
typedef float v2f __attribute__((ext_vector_type(2)));
typedef __bf16 bf16x2 __attribute__((ext_vector_type(2)));
#define GAS __attribute__((address_space(1)))
typedef GAS unsigned long long gu64;
typedef GAS unsigned gu32;
#define RLX_AGENT __ATOMIC_RELAXED, __HIP_MEMORY_SCOPE_AGENT

constexpr int SK = 65536, WIN = 4096, FF = 1792;
constexpr int NWG = 256, NTHR = 576;
constexpr unsigned WS_EPOCH = 0;
constexpr unsigned WS_E1 = 4096;
constexpr unsigned WS_E2O = WS_E1 + 8 * 256 * 8;
constexpr unsigned WS_E2ML = WS_E2O + 8 * 8 * 8 * 32 * 8 * 8;
constexpr unsigned WS_E3A = WS_E2ML + 8 * 4 * 32 * 2 * 8;
constexpr unsigned WS_E3B = WS_E3A + 64 * 64 * 8 * 8;
constexpr unsigned WS_E5 = WS_E3B + 8 * 256 * 8;
constexpr unsigned WS_END = WS_E5 + 64 * 64 * 8 * 8;
constexpr unsigned SPIN_CAP = 1u << 22;

__device__ __forceinline__ float asf(unsigned u) { return __builtin_bit_cast(float, u); }
__device__ __forceinline__ unsigned asu(float f) { return __builtin_bit_cast(unsigned, f); }
__device__ __forceinline__ float bf_lo(unsigned u) { return __builtin_bit_cast(float, u << 16); }
__device__ __forceinline__ float bf_hi(unsigned u) { return __builtin_bit_cast(float, u & 0xffff0000u); }
__device__ __forceinline__ unsigned f2bf(float f) {
    unsigned u = __builtin_bit_cast(unsigned, f);
    return (u + 0x7fffu + ((u >> 16) & 1u)) >> 16;
}
__device__ __forceinline__ float rbf(float f) { return __builtin_bit_cast(float, f2bf(f) << 16); }
template <int CTRL> __device__ __forceinline__ float dpp(float x) {
    return __builtin_bit_cast(float, __builtin_amdgcn_mov_dpp(__builtin_bit_cast(int, x), CTRL, 0xf, 0xf, true));
}
constexpr int XOR1 = 0xB1, XOR2 = 0x4E, XOR7 = 0x141, ROR4 = 0x124, ROR8 = 0x128;
__device__ __forceinline__ float swap32_add(float a, float b) {
    auto s = __builtin_amdgcn_permlane32_swap(asu(a), asu(b), false, false);
    return asf(s[0]) + asf(s[1]);
}
__device__ __forceinline__ float swap16_add(float a, float b) {
    auto s = __builtin_amdgcn_permlane16_swap(asu(a), asu(b), false, false);
    return asf(s[0]) + asf(s[1]);
}
__device__ __forceinline__ float swap32_max(float a) {
    auto s = __builtin_amdgcn_permlane32_swap(asu(a), asu(a), false, false);
    return fmaxf(asf(s[0]), asf(s[1]));
}
__device__ __forceinline__ float swap16_max(float a) {
    auto s = __builtin_amdgcn_permlane16_swap(asu(a), asu(a), false, false);
    return fmaxf(asf(s[0]), asf(s[1]));
}
__device__ __forceinline__ float wave_sum(float v) {
    v += dpp<XOR1>(v); v += dpp<XOR2>(v); v += dpp<ROR4>(v); v += dpp<ROR8>(v);
    v = swap16_add(v, v); v = swap32_add(v, v);
    return v;
}
__device__ __forceinline__ float wave_max(float v) {
    v = fmaxf(v, dpp<XOR1>(v)); v = fmaxf(v, dpp<XOR2>(v)); v = fmaxf(v, dpp<ROR4>(v)); v = fmaxf(v, dpp<ROR8>(v));
    v = swap16_max(v); v = swap32_max(v);
    return v;
}
__device__ __forceinline__ float sum_hi4(float v) {
    v += dpp<ROR4>(v); v += dpp<ROR8>(v);
    v = swap16_add(v, v); v = swap32_add(v, v);
    return v;
}
__device__ __forceinline__ float dot2(unsigned a, unsigned b, float acc) {
    return __builtin_amdgcn_fdot2_f32_bf16(__builtin_bit_cast(bf16x2, a), __builtin_bit_cast(bf16x2, b), acc, false);
}
__device__ __forceinline__ float rdl(float v, int l) { return asf(__builtin_amdgcn_readlane(asu(v), l)); }
#define BAR() asm volatile("s_waitcnt lgkmcnt(0)\n\ts_barrier" ::: "memory")
#define STAMP(i) do { } while (0)
#define SB() do { asm volatile("" ::: "memory"); __builtin_amdgcn_sched_barrier(0); } while (0)
#define LDG(p) __builtin_nontemporal_load((const v4u*)(p))
#define LD16(off) __builtin_amdgcn_raw_buffer_load_b128(rs, (off), 0, 16)
#define ST16(off, a, b) __builtin_amdgcn_raw_buffer_store_b128(v4u{(a), tag, (b), tag}, rs, (off), 0, 16)
#define TAGOK(v) (((v).y == tag) & ((v).w == tag))
#define SPIN_END() if (__all(ok) || spin > SPIN_CAP) break; asm volatile("" ::: "memory")

__device__ __forceinline__ void st_gran(unsigned char* ws, unsigned off, unsigned tag, unsigned val) {
    __hip_atomic_store((gu64*)(ws + off), ((unsigned long long)tag << 32) | val, RLX_AGENT);
}

__device__ __forceinline__ void ln_stats(const float (&v)[8], float& m, float& sd) {
    float s = ((v[0] + v[1]) + (v[2] + v[3])) + ((v[4] + v[5]) + (v[6] + v[7]));
    s = wave_sum(s);
    const float mean = s * (1.f / 512.f);
    float q = 0.f;
#pragma unroll
    for (int i = 0; i < 8; ++i) { const float d = v[i] - mean; q = fmaf(d, d, q); }
    q = wave_sum(q);
    m = rbf(mean);
    sd = rbf(sqrtf(rbf(rbf(q * (1.f / 512.f)) + 1e-5f)));
}
__device__ __forceinline__ void fma8(float (&a)[8], float sc, const v4u& w) {
    a[0] = fmaf(sc, bf_lo(w.x), a[0]); a[1] = fmaf(sc, bf_hi(w.x), a[1]); a[2] = fmaf(sc, bf_lo(w.y), a[2]); a[3] = fmaf(sc, bf_hi(w.y), a[3]);
    a[4] = fmaf(sc, bf_lo(w.z), a[4]); a[5] = fmaf(sc, bf_hi(w.z), a[5]); a[6] = fmaf(sc, bf_lo(w.w), a[6]); a[7] = fmaf(sc, bf_hi(w.w), a[7]);
}

struct Lds {
    float big[8 * 512];
    unsigned qs[256];
    float pb[8 * 64];
    float ml[4 * 8 * 2];
    float red1[8 * 16];
    float red4[32 * 8];
};

__global__ void __launch_bounds__(NTHR) fused_block(const u16* __restrict__ x, const u16* __restrict__ kc, const u16* __restrict__ vc,
                                                     const u16* __restrict__ wq, const u16* __restrict__ wo, const u16* __restrict__ w1,
                                                     const u16* __restrict__ b1, const u16* __restrict__ w2, const u16* __restrict__ b2,
                                                     u16* __restrict__ out, unsigned char* ws) {
    __shared__ __attribute__((aligned(16))) Lds L;
    const int tid = threadIdx.x, lane = tid & 63, wave = __builtin_amdgcn_readfirstlane(tid >> 6);
    const int b = blockIdx.x, g = b & 7, s = b >> 3;
    const bool is_wo = s < 8, is_mlp = (s >= 8) & (s < 15), is_red = (s >= 16) & (s < 24);
    const __amdgpu_buffer_rsrc_t rs = __builtin_amdgcn_make_buffer_rsrc(ws, 0, WS_END, 0x00020000);
    STAMP(0);

    if (wave < 8) {
        const int r = lane >> 4, c = lane & 15;
        const int r0 = tid >> 1, hf = tid & 1;
        const u16* wqp = wq + (size_t)r0 * 4096 + 512 * g + 16 * s + 8 * hf;
        const size_t kvoff = ((size_t)g * SK + (SK - WIN) + 128 * s + 16 * wave + r) * 128 + 8 * c;
        const int j = g * 7 + (s - 8);
        const u16* rp0; const u16* rp1; size_t st0, st1;
        if (is_mlp) { rp0 = w1 + (size_t)(64 * wave + c) * FF + 32 * j + 8 * r; st0 = (size_t)16 * FF; rp1 = w2 + (size_t)(32 * j + 4 * wave) * 512 + 8 * lane; st1 = 512; }
        else if (is_wo) { rp0 = wo + (size_t)(512 * g + 64 * s + 8 * wave) * 512 + 8 * lane; st0 = 512; rp1 = rp0 + 4 * 512; st1 = 512; }
        else { rp0 = x + 8 * lane; st0 = 0; rp1 = rp0; st1 = 0; }
        const u16* b1p = is_mlp ? b1 + 32 * j + 4 * wave + 2 * (lane & 1) : x;
        SB();
        const v4u xv = *(const v4u*)(x + 8 * lane);
        const unsigned xr0 = x[r0], xr1 = x[r0 + 256];
        const unsigned epoch = __hip_atomic_load((gu32*)(ws + WS_EPOCH), RLX_AGENT);
        SB();
        const v4u q0 = LDG(wqp), q1 = LDG(wqp + (size_t)256 * 4096);
        SB();
        v4u K[4], V[4], R[8];
#pragma unroll
        for (int i = 0; i < 4; ++i) K[i] = LDG(kc + kvoff + i * 512);
        SB();
        {
            float xf[8] = {bf_lo(xv.x), bf_hi(xv.x), bf_lo(xv.y), bf_hi(xv.y), bf_lo(xv.z), bf_hi(xv.z), bf_lo(xv.w), bf_hi(xv.w)};
            float m, sd;
            ln_stats(xf, m, sd);
            const float n0 = rbf(rbf(bf_lo(xr0) - m) / sd), n1 = rbf(rbf(bf_lo(xr1) - m) / sd);
            float a[8] = {0.f, 0.f, 0.f, 0.f, 0.f, 0.f, 0.f, 0.f};
            fma8(a, n0, q0); fma8(a, n1, q1);
            float u[4];
#pragma unroll
            for (int k = 0; k < 4; ++k) u[k] = swap32_add(a[k], a[k + 4]);
            const float z0 = swap16_add(u[0], u[2]), z1 = swap16_add(u[1], u[3]);
            const float t0 = z0 + dpp<XOR2>(z0), t1 = z1 + dpp<XOR2>(z1);
            float rr = (lane & 2) ? t1 : t0;
            rr += dpp<ROR4>(rr); rr += dpp<ROR8>(rr);
            if ((lane & 12) == 0) L.red1[wave * 16 + 8 * (lane & 1) + ((lane >> 1) & 1) + 2 * ((lane >> 4) & 1) + 4 * (lane >> 5)] = rr;
        }
        SB();
        STAMP(11);
        BAR();
        BAR();
#pragma unroll
        for (int i = 0; i < 4; ++i) V[i] = LDG(vc + kvoff + i * 512);
        SB();
        BAR();
        {
            v4u qp[4];
#pragma unroll
            for (int h = 0; h < 4; ++h) qp[h] = *(const v4u*)&L.qs[h * 64 + 4 * c];
            float xs[16];
#pragma unroll
            for (int i = 0; i < 4; ++i)
#pragma unroll
                for (int h = 0; h < 4; ++h) {
                    float d = dot2(K[i].x, qp[h].x, 0.f);
                    d = dot2(K[i].y, qp[h].y, d); d = dot2(K[i].z, qp[h].z, d); d = dot2(K[i].w, qp[h].w, d);
                    xs[i * 4 + h] = d;
                }
#define TR_STEP(HALF, CTRL, BIT) _Pragma("unroll") for (int h = 0; h < (HALF); ++h) { \
            const float lo_ = xs[h], hi_ = xs[h + (HALF)]; const float t1_ = lo_ + dpp<CTRL>(lo_), t2_ = hi_ + dpp<CTRL>(hi_); \
            xs[h] = (c & (BIT)) ? t2_ : t1_; }
            TR_STEP(8, ROR8, 8) TR_STEP(4, XOR7, 4) TR_STEP(2, XOR2, 2) TR_STEP(1, XOR1, 1)
            const float sc = rbf(rbf(xs[0]) * 0.08838834764831845f);
            float m = fmaxf(sc, dpp<ROR4>(sc)); m = fmaxf(m, dpp<ROR8>(m)); m = swap16_max(m); m = swap32_max(m);
            const float p = __expf(sc - m);
            float l = p + dpp<ROR4>(p); l += dpp<ROR8>(l); l = swap16_add(l, l); l = swap32_add(l, l);
            L.pb[wave * 64 + lane] = p;
            if (lane < 4) { L.ml[(lane * 8 + wave) * 2] = m; L.ml[(lane * 8 + wave) * 2 + 1] = l; }
            asm volatile("s_waitcnt lgkmcnt(0)" ::: "memory");
            v4f pr[4];
#pragma unroll
            for (int i = 0; i < 4; ++i) pr[i] = *(const v4f*)&L.pb[wave * 64 + r * 16 + 4 * i];
            float acc[4][8];
#pragma unroll
            for (int h = 0; h < 4; ++h)
#pragma unroll
                for (int k = 0; k < 8; ++k) acc[h][k] = 0.f;
#pragma unroll
            for (int i = 0; i < 4; ++i)
#pragma unroll
                for (int h = 0; h < 4; ++h) fma8(acc[h], pr[i][h], V[i]);
            float fo[8];
#pragma unroll
            for (int k = 0; k < 8; ++k) {
                const float e0 = swap32_add(acc[0][k], acc[2][k]), e1 = swap32_add(acc[1][k], acc[3][k]);
                fo[k] = swap16_add(e0, e1);
            }
            float* dst = &L.big[wave * 512 + r * 128 + 8 * c];
            *(v4f*)dst = v4f{fo[0], fo[1], fo[2], fo[3]};
            *(v4f*)(dst + 4) = v4f{fo[4], fo[5], fo[6], fo[7]};
        }
        BAR();
        const unsigned tag = epoch + 1u;
        if (wave < 4) {
            const int h = wave, d = 2 * lane;
            const v4f ma = *(const v4f*)&L.ml[h * 16], mb = *(const v4f*)&L.ml[h * 16 + 4], mc = *(const v4f*)&L.ml[h * 16 + 8], md = *(const v4f*)&L.ml[h * 16 + 12];
            const float mw[8] = {ma.x, ma.z, mb.x, mb.z, mc.x, mc.z, md.x, md.z};
            const float lw[8] = {ma.y, ma.w, mb.y, mb.w, mc.y, mc.w, md.y, md.w};
            float M = mw[0];
#pragma unroll
            for (int w = 1; w < 8; ++w) M = fmaxf(M, mw[w]);
            float oa = 0.f, ob = 0.f, Ls = 0.f;
#pragma unroll
            for (int w = 0; w < 8; ++w) { const float e = __expf(mw[w] - M); const v2f t = *(const v2f*)&L.big[w * 512 + 2 * tid]; oa = fmaf(e, t.x, oa); ob = fmaf(e, t.y, ob); Ls = fmaf(e, lw[w], Ls); }
            ST16(WS_E2O + ((((g * 8 + 2 * h + (d >> 6)) * 8 + ((d >> 3) & 7)) * 32 + s) * 8 + (d & 7)) * 8, asu(oa), asu(ob));
            if (lane == 0) ST16(WS_E2ML + ((g * 4 + h) * 32 + s) * 16, asu(M), asu(Ls));
        }
        STAMP(3);
        if (!(is_wo | is_mlp)) {
            return;
        }
        BAR();
#pragma unroll
        for (int i = 0; i < 4; ++i) R[i] = LDG(rp0 + i * st0);
#pragma unroll
        for (int i = 0; i < 4; ++i) R[4 + i] = LDG(rp1 + i * st1);
        const unsigned b1v = *(const unsigned*)b1p;
        SB();
        if (is_wo) {
            v4u v[2], mlv;
            for (unsigned spin = 0;; ++spin) {
                bool ok = true;
#pragma unroll
                for (int k = 0; k < 2; ++k) { v[k] = LD16(WS_E2O + ((g * 8 + s) * 8 + wave) * 2048 + k * 1024 + lane * 16); ok &= TAGOK(v[k]); }
                mlv = LD16(WS_E2ML + (g * 4 + (s >> 1)) * 512 + (lane & 31) * 16);
                ok &= TAGOK(mlv);
                SPIN_END();
            }
            const float ms = asf(mlv.x), ls = asf(mlv.z);
            const float M = wave_max(ms);
            const float wgt = __expf(ms - M);
            const float Lt = 0.5f * wave_sum(wgt * ls);
            const float wa = __shfl(wgt, lane >> 2), wb = __shfl(wgt, (lane >> 2) + 16);
            float a0 = fmaf(wb, asf(v[1].x), wa * asf(v[0].x)), a1 = fmaf(wb, asf(v[1].z), wa * asf(v[0].z));
            a0 = rbf(sum_hi4(a0) / Lt); a1 = rbf(sum_hi4(a1) / Lt);
            float acc[8] = {0.f, 0.f, 0.f, 0.f, 0.f, 0.f, 0.f, 0.f};
#pragma unroll
            for (int i = 0; i < 4; ++i) { fma8(acc, rdl(a0, i), R[2 * i]); fma8(acc, rdl(a1, i), R[2 * i + 1]); }
            STAMP(4);
            float* dst = &L.big[wave * 512 + 8 * lane];
            *(v4f*)dst = v4f{acc[0], acc[1], acc[2], acc[3]};
            *(v4f*)(dst + 4) = v4f{acc[4], acc[5], acc[6], acc[7]};
            BAR();
            if (wave < 4) {
                float s0 = 0.f, s1 = 0.f;
#pragma unroll
                for (int w = 0; w < 8; ++w) { const v2f t = *(const v2f*)&L.big[w * 512 + 2 * tid]; s0 += t.x; s1 += t.y; }
                ST16(WS_E3A + (((tid >> 2) * 64 + (g * 8 + s)) * 8 + 2 * (tid & 3)) * 8, asu(s0), asu(s1));
            }
            STAMP(5);
        } else if (is_mlp) {
            v4u v[2]; v2u hr[4];
            for (unsigned spin = 0;; ++spin) {
                bool ok = true;
#pragma unroll
                for (int k = 0; k < 2; ++k) { v[k] = LD16(WS_E3B + g * 2048 + k * 1024 + lane * 16); ok &= TAGOK(v[k]); }
#pragma unroll
                for (int i = 0; i < 4; ++i) { hr[i] = __builtin_amdgcn_raw_buffer_load_b64(rs, WS_E3B + g * 2048 + ((64 * wave + 16 * i + c) >> 1) * 8, 0, 16); ok &= (hr[i].y == tag); }
                SPIN_END();
            }
            STAMP(6);
            float hf[8] = {bf_lo(v[0].x), bf_hi(v[0].x), bf_lo(v[0].z), bf_hi(v[0].z), bf_lo(v[1].x), bf_hi(v[1].x), bf_lo(v[1].z), bf_hi(v[1].z)};
            float m, sd;
            ln_stats(hf, m, sd);
            float xs[8] = {0.f, 0.f, 0.f, 0.f, 0.f, 0.f, 0.f, 0.f};
#pragma unroll
            for (int i = 0; i < 4; ++i) { const float hv = (c & 1) ? bf_hi(hr[i].x) : bf_lo(hr[i].x); fma8(xs, rbf(rbf(hv - m) / sd), R[i]); }
            TR_STEP(4, ROR8, 8) TR_STEP(2, XOR7, 4) TR_STEP(1, XOR2, 2)
            float rr = xs[0] + dpp<XOR1>(xs[0]);
            if ((c & 1) == 0) L.red4[(8 * r + ((c >> 3) & 1) * 4 + ((c >> 2) & 1) * 2 + ((c >> 1) & 1)) * 8 + wave] = rr;
            BAR();
            float acc[8] = {0.f, 0.f, 0.f, 0.f, 0.f, 0.f, 0.f, 0.f};
#pragma unroll
            for (int i = 0; i < 4; ++i) {
                const v4f ra = *(const v4f*)&L.red4[(4 * wave + i) * 8], rb = *(const v4f*)&L.red4[(4 * wave + i) * 8 + 4];
                const float sum = ((ra.x + ra.y) + (ra.z + ra.w)) + ((rb.x + rb.y) + (rb.z + rb.w));
                const unsigned bw = __builtin_amdgcn_readlane(b1v, i >> 1);
                const float u = rbf(rbf(sum) + ((i & 1) ? bf_hi(bw) : bf_lo(bw)));
                const float z = 0.7978845608028654f * (u + 0.044715f * u * u * u);
                const float th = 1.f - 2.f / (__expf(2.f * z) + 1.f);
                fma8(acc, rbf(0.5f * u * (1.f + th)), R[4 + i]);
            }
            float* dst = &L.big[wave * 512 + 8 * lane];
            *(v4f*)dst = v4f{acc[0], acc[1], acc[2], acc[3]};
            *(v4f*)(dst + 4) = v4f{acc[4], acc[5], acc[6], acc[7]};
            BAR();
            if (wave < 4) {
                float s0 = 0.f, s1 = 0.f;
#pragma unroll
                for (int w = 0; w < 8; ++w) { const v2f t = *(const v2f*)&L.big[w * 512 + 2 * tid]; s0 += t.x; s1 += t.y; }
                ST16(WS_E5 + (((tid >> 2) * 64 + j) * 8 + 2 * (tid & 3)) * 8, asu(s0), asu(s1));
            }
            STAMP(7);
        }
        return;
    }
#undef TR_STEP
    {
        const unsigned tag = __hip_atomic_load((gu32*)(ws + WS_EPOCH), RLX_AGENT) + 1u;
        const int rj = g * 8 + (s - 16);
        const int rc = is_red ? 8 * rj + 2 * (lane & 3) : 0;
        const unsigned xpair = *(const unsigned*)(x + rc);
        const unsigned b2pair = *(const unsigned*)(b2 + rc);
        {
            const int jj = is_mlp ? g * 7 + (s - 8) : 0;
            const u16* tp;
            const size_t kvo = ((size_t)g * SK + (SK - WIN) + 128 * s) * 128;
            switch (lane & 7) {
                case 0: tp = kc + kvo; break;
                case 1: tp = vc + kvo; break;
                case 2: tp = wo + (size_t)(512 * g + 64 * (s & 7)) * 512; break;
                case 3: tp = w1 + 32 * jj; break;
                case 4: tp = w1 + (size_t)511 * FF + 32 * jj; break;
                case 5: tp = w2 + (size_t)(32 * jj) * 512; break;
                case 6: tp = (const u16*)(ws + WS_E2O + g * 131072); break;
                default: tp = (const u16*)(ws + WS_E5); break;
            }
            unsigned tv = __builtin_nontemporal_load((const unsigned*)tp);
            asm volatile("" :: "v"(tv));
        }
        BAR();
        STAMP(1);
        if (lane < 8) {
            float a0 = 0.f, a1 = 0.f;
#pragma unroll
            for (int w = 0; w < 8; ++w) { const v2f t = *(const v2f*)&L.red1[w * 16 + 2 * lane]; a0 += t.x; a1 += t.y; }
            st_gran(ws, WS_E1 + (g * 256 + s * 8 + lane) * 8, tag, f2bf(a0) | (f2bf(a1) << 16));
        }
        BAR();
        {
            v4u v[2];
            for (unsigned spin = 0;; ++spin) {
                bool ok = true;
#pragma unroll
                for (int k = 0; k < 2; ++k) { v[k] = LD16(WS_E1 + g * 2048 + k * 1024 + lane * 16); ok &= TAGOK(v[k]); }
                SPIN_END();
            }
            *(v2u*)&L.qs[2 * lane] = v2u{v[0].x, v[0].z};
            *(v2u*)&L.qs[128 + 2 * lane] = v2u{v[1].x, v[1].z};
        }
        STAMP(2);
        BAR();
        if (is_red) {
            BAR();
            float h0, h1;
            {
                v4u v[4];
                for (unsigned spin = 0;; ++spin) {
                    bool ok = true;
#pragma unroll
                    for (int k = 0; k < 4; ++k) { v[k] = LD16(WS_E3A + rj * 4096 + k * 1024 + lane * 16); ok &= TAGOK(v[k]); }
                    SPIN_END();
                }
                float a0 = (asf(v[0].x) + asf(v[1].x)) + (asf(v[2].x) + asf(v[3].x));
                float a1 = (asf(v[0].z) + asf(v[1].z)) + (asf(v[2].z) + asf(v[3].z));
                a0 = sum_hi4(a0); a1 = sum_hi4(a1);
                h0 = rbf(bf_lo(xpair) + rbf(a0)); h1 = rbf(bf_hi(xpair) + rbf(a1));
                if (lane < 32) st_gran(ws, WS_E3B + ((lane >> 2) * 256 + 4 * rj + (lane & 3)) * 8, tag, f2bf(h0) | (f2bf(h1) << 16));
            }
            STAMP(8);
            {
                v4u v[4];
                for (unsigned spin = 0;; ++spin) {
                    bool ok = true;
#pragma unroll
                    for (int k = 0; k < 4; ++k) { const int o = (k < 3 || lane < 32) ? k * 1024 + lane * 16 : lane * 16; v[k] = LD16(WS_E5 + rj * 4096 + o); ok &= TAGOK(v[k]); }
                    SPIN_END();
                }
                const float l0 = lane < 32 ? asf(v[3].x) : 0.f, l1 = lane < 32 ? asf(v[3].z) : 0.f;
                float a0 = (asf(v[0].x) + asf(v[1].x)) + (asf(v[2].x) + l0);
                float a1 = (asf(v[0].z) + asf(v[1].z)) + (asf(v[2].z) + l1);
                a0 = sum_hi4(a0); a1 = sum_hi4(a1);
                const float y0 = rbf(rbf(a0) + bf_lo(b2pair)), y1 = rbf(rbf(a1) + bf_hi(b2pair));
                if (lane < 4) *(unsigned*)(out + rc) = f2bf(h0 + y0) | (f2bf(h1 + y1) << 16);
                if (lane == 0 && rj == 0) __hip_atomic_store((gu32*)(ws + WS_EPOCH), tag, RLX_AGENT);
            }
            STAMP(9);
        }
    }
}

extern "C" void kernel_launch(void* const* d_in, const int* in_sizes, int n_in, void* d_out, int out_size, void* d_ws, size_t ws_size, hipStream_t stream) {
    (void)in_sizes; (void)n_in; (void)out_size; (void)ws_size;
    fused_block<<<NWG, NTHR, 0, stream>>>((const u16*)d_in[0], (const u16*)d_in[1], (const u16*)d_in[2], (const u16*)d_in[3], (const u16*)d_in[4],
                                          (const u16*)d_in[6], (const u16*)d_in[7], (const u16*)d_in[8], (const u16*)d_in[9], (u16*)d_out, (unsigned char*)d_ws);
}
```

```cpp
#include <hip/hip_runtime.h>
#include <stdint.h>

typedef unsigned short u16;
typedef unsigned v4u __attribute__((ext_vector_type(4)));
typedef unsigned v2u __attribute__((ext_vector_type(2)));
typedef float v4f __attribute__((ext_vector_type(4)));
typedef float v2f __attribute__((ext_vector_type(2)));
typedef __bf16 bf16x2 __attribute__((ext_vector_type(2)));
typedef __bf16 bf16x8 __attribute__((ext_vector_type(8)));
#define GAS __attribute__((address_space(1)))
typedef GAS unsigned long long gu64;
typedef GAS unsigned gu32;
#define RLX_AGENT __ATOMIC_RELAXED, __HIP_MEMORY_SCOPE_AGENT

constexpr int SK = 65536, WIN = 4096, FF = 1792;
constexpr int NWG = 256, NTHR = 576;
constexpr unsigned WS_EPOCH = 0;
constexpr unsigned WS_E1 = 4096;
constexpr unsigned WS_E2O = WS_E1 + 8 * 256 * 8;
constexpr unsigned WS_E2ML = WS_E2O + 8 * 8 * 8 * 32 * 8 * 8;
constexpr unsigned WS_E3A = WS_E2ML + 8 * 4 * 32 * 2 * 8;
constexpr unsigned WS_E3B = WS_E3A + 64 * 64 * 8 * 8;
constexpr unsigned WS_E5 = WS_E3B + 8 * 256 * 8;
constexpr unsigned WS_END = WS_E5 + 64 * 64 * 8 * 8;
constexpr unsigned SPIN_CAP = 1u << 22;

__device__ __forceinline__ float asf(unsigned u) { return __builtin_bit_cast(float, u); }
__device__ __forceinline__ unsigned asu(float f) { return __builtin_bit_cast(unsigned, f); }
__device__ __forceinline__ float bf_lo(unsigned u) { return __builtin_bit_cast(float, u << 16); }
__device__ __forceinline__ float bf_hi(unsigned u) { return __builtin_bit_cast(float, u & 0xffff0000u); }
__device__ __forceinline__ unsigned cvtpk(float lo, float hi) {
    const bf16x2 v = {(__bf16)lo, (__bf16)hi};
    return __builtin_bit_cast(unsigned, v);
}
__device__ __forceinline__ float rbf(float f) { return bf_lo(cvtpk(f, f)); }
template <int CTRL> __device__ __forceinline__ float dpp(float x) {
    return __builtin_bit_cast(float, __builtin_amdgcn_mov_dpp(__builtin_bit_cast(int, x), CTRL, 0xf, 0xf, true));
}
constexpr int XOR1 = 0xB1, XOR2 = 0x4E, XOR7 = 0x141, ROR4 = 0x124, ROR8 = 0x128;
__device__ __forceinline__ float swap32_add(float a, float b) {
    auto s = __builtin_amdgcn_permlane32_swap(asu(a), asu(b), false, false);
    return asf(s[0]) + asf(s[1]);
}
__device__ __forceinline__ float swap16_add(float a, float b) {
    auto s = __builtin_amdgcn_permlane16_swap(asu(a), asu(b), false, false);
    return asf(s[0]) + asf(s[1]);
}
__device__ __forceinline__ float swap32_max(float a) {
    auto s = __builtin_amdgcn_permlane32_swap(asu(a), asu(a), false, false);
    return fmaxf(asf(s[0]), asf(s[1]));
}
__device__ __forceinline__ float swap16_max(float a) {
    auto s = __builtin_amdgcn_permlane16_swap(asu(a), asu(a), false, false);
    return fmaxf(asf(s[0]), asf(s[1]));
}
__device__ __forceinline__ float row_sum(float v) { v += dpp<XOR1>(v); v += dpp<XOR2>(v); v += dpp<ROR4>(v); v += dpp<ROR8>(v); return v; }
__device__ __forceinline__ float row_max(float v) { v = fmaxf(v, dpp<XOR1>(v)); v = fmaxf(v, dpp<XOR2>(v)); v = fmaxf(v, dpp<ROR4>(v)); v = fmaxf(v, dpp<ROR8>(v)); return v; }
__device__ __forceinline__ float wave_sum(float v) { v = row_sum(v); v = swap16_add(v, v); v = swap32_add(v, v); return v; }
__device__ __forceinline__ float wave_max(float v) { v = row_max(v); v = swap16_max(v); v = swap32_max(v); return v; }
__device__ __forceinline__ float sum_hi4(float v) {
    v += dpp<ROR4>(v); v += dpp<ROR8>(v);
    v = swap16_add(v, v); v = swap32_add(v, v);
    return v;
}
__device__ __forceinline__ float dot2(unsigned a, unsigned b, float acc) {
    return __builtin_amdgcn_fdot2_f32_bf16(__builtin_bit_cast(bf16x2, a), __builtin_bit_cast(bf16x2, b), acc, false);
}
__device__ __forceinline__ unsigned plo(unsigned a, unsigned b) { return __builtin_amdgcn_perm(b, a, 0x05040100u); }
__device__ __forceinline__ unsigned phi(unsigned a, unsigned b) { return __builtin_amdgcn_perm(b, a, 0x07060302u); }
__device__ __forceinline__ void dot2x8(float (&a)[8], unsigned sp, const v4u& A, const v4u& B) {
    a[0] = dot2(plo(A.x, B.x), sp, a[0]); a[1] = dot2(phi(A.x, B.x), sp, a[1]);
    a[2] = dot2(plo(A.y, B.y), sp, a[2]); a[3] = dot2(phi(A.y, B.y), sp, a[3]);
    a[4] = dot2(plo(A.z, B.z), sp, a[4]); a[5] = dot2(phi(A.z, B.z), sp, a[5]);
    a[6] = dot2(plo(A.w, B.w), sp, a[6]); a[7] = dot2(phi(A.w, B.w), sp, a[7]);
}
#define BAR() asm volatile("s_waitcnt lgkmcnt(0)\n\ts_barrier" ::: "memory")
#define STAMP(i) do { } while (0)
#define STAMPOUT(lo, hi) do { } while (0)
#define SB() do { asm volatile("" ::: "memory"); __builtin_amdgcn_sched_barrier(0); } while (0)
#define LDG(p) __builtin_nontemporal_load((const v4u*)(p))
#define LD16(off) __builtin_amdgcn_raw_buffer_load_b128(rs, (off), 0, 16)
#define ST16(off, a, b) __builtin_amdgcn_raw_buffer_store_b128(v4u{(a), tag, (b), tag}, rs, (off), 0, 16)
#define TAGOK(v) (((v).y == tag) & ((v).w == tag))
#define SPIN_END() if (__all(ok) || spin > SPIN_CAP) break; asm volatile("" ::: "memory")

__device__ __forceinline__ void st_gran(unsigned char* ws, unsigned off, unsigned tag, unsigned val) {
    __hip_atomic_store((gu64*)(ws + off), ((unsigned long long)tag << 32) | val, RLX_AGENT);
}

__device__ __forceinline__ void ln_stats(const float (&v)[8], float& m, float& rsd) {
    float s = ((v[0] + v[1]) + (v[2] + v[3])) + ((v[4] + v[5]) + (v[6] + v[7]));
    s = wave_sum(s);
    const float mean = s * (1.f / 512.f);
    float q = 0.f;
#pragma unroll
    for (int i = 0; i < 8; ++i) { const float d = v[i] - mean; q = fmaf(d, d, q); }
    q = wave_sum(q);
    m = rbf(mean);
    rsd = __builtin_amdgcn_rcpf(rbf(__builtin_amdgcn_sqrtf(rbf(rbf(q * (1.f / 512.f)) + 1e-5f))));
}

struct Lds {
    float big[8 * 512];
    unsigned qs[256];
    unsigned pb[8 * 32];
    float ml[4 * 8 * 2];
    float red1[8 * 16];
    float red4[32 * 8];
};

__global__ void __launch_bounds__(NTHR) fused_block(const u16* __restrict__ x, const u16* __restrict__ kc, const u16* __restrict__ vc,
                                                     const u16* __restrict__ wq, const u16* __restrict__ wo, const u16* __restrict__ w1,
                                                     const u16* __restrict__ b1, const u16* __restrict__ w2, const u16* __restrict__ b2,
                                                     u16* __restrict__ out, unsigned char* ws) {
    __shared__ __attribute__((aligned(16))) Lds L;
    const int tid = threadIdx.x, lane = tid & 63, wave = __builtin_amdgcn_readfirstlane(tid >> 6);
    const int b = blockIdx.x, g = b & 7, s = b >> 3;
    const bool is_wo = s < 8, is_mlp = (s >= 8) & (s < 15), is_red = (s >= 16) & (s < 24);
    const __amdgpu_buffer_rsrc_t rs = __builtin_amdgcn_make_buffer_rsrc(ws, 0, WS_END, 0x00020000);
    STAMP(0);

    if (wave < 8) {
        const int r = lane >> 4, c = lane & 15;
        const int r0 = tid >> 1, hf = tid & 1;
        const u16* wqp = wq + (size_t)r0 * 4096 + 512 * g + 16 * s + 8 * hf;
        const size_t kvrow = (size_t)g * SK + (SK - WIN) + 128 * s + 16 * wave;
        const u16* kp = kc + (kvrow + c) * 128 + 8 * r;
        const u16* vp = vc + kvrow * 128 + 2 * lane;
        SB();
        const v4u xv = *(const v4u*)(x + 8 * lane);
        const unsigned xr0 = x[r0], xr1 = x[r0 + 256];
        const unsigned epoch = __hip_atomic_load((gu32*)(ws + WS_EPOCH), RLX_AGENT);
        SB();
        const v4u q0 = LDG(wqp), q1 = LDG(wqp + (size_t)256 * 4096);
        SB();
        v4u K[4];
#pragma unroll
        for (int i = 0; i < 4; ++i) K[i] = LDG(kp + i * 32);
        SB();
        {
            float xf[8] = {bf_lo(xv.x), bf_hi(xv.x), bf_lo(xv.y), bf_hi(xv.y), bf_lo(xv.z), bf_hi(xv.z), bf_lo(xv.w), bf_hi(xv.w)};
            float m, rsd;
            ln_stats(xf, m, rsd);
            const unsigned np = cvtpk(rbf(bf_lo(xr0) - m) * rsd, rbf(bf_lo(xr1) - m) * rsd);
            float a[8] = {0.f, 0.f, 0.f, 0.f, 0.f, 0.f, 0.f, 0.f};
            dot2x8(a, np, q0, q1);
            float u[4];
#pragma unroll
            for (int k = 0; k < 4; ++k) u[k] = swap32_add(a[k], a[k + 4]);
            const float z0 = swap16_add(u[0], u[2]), z1 = swap16_add(u[1], u[3]);
            const float t0 = z0 + dpp<XOR2>(z0), t1 = z1 + dpp<XOR2>(z1);
            float rr = (lane & 2) ? t1 : t0;
            rr += dpp<ROR4>(rr); rr += dpp<ROR8>(rr);
            if ((lane & 12) == 0) L.red1[wave * 16 + 8 * (lane & 1) + ((lane >> 1) & 1) + 2 * ((lane >> 4) & 1) + 4 * (lane >> 5)] = rr;
        }
        SB();
        STAMP(11);
        BAR();
        BAR();
        unsigned V[16];
#pragma unroll
        for (int i = 0; i < 16; ++i) V[i] = __builtin_nontemporal_load((const unsigned*)(vp + i * 128));
        SB();
        BAR();
        {
            v4f sacc = {0.f, 0.f, 0.f, 0.f};
#pragma unroll
            for (int ks = 0; ks < 4; ++ks) {
                const v4u qa = *(const v4u*)&L.qs[(c >> 2) * 64 + 16 * ks + 4 * r];
                sacc = __builtin_amdgcn_mfma_f32_16x16x32_bf16(__builtin_bit_cast(bf16x8, qa), __builtin_bit_cast(bf16x8, K[ks]), sacc, 0, 0, 0);
            }
            const float sc = rbf(rbf(sacc[0]) * 0.08838834764831845f);
            const float m = row_max(sc);
            const float p = __expf(sc - m);
            const float l = row_sum(p);
            const unsigned pp = cvtpk(p, dpp<XOR1>(p));
            if ((c & 1) == 0) L.pb[wave * 32 + r * 8 + (c >> 1)] = pp;
            if (c == 0) *(v2f*)&L.ml[(r * 8 + wave) * 2] = v2f{m, l};
            asm volatile("s_waitcnt lgkmcnt(0)" ::: "memory");
            v4u P[8];
#pragma unroll
            for (int i = 0; i < 8; ++i) P[i] = *(const v4u*)&L.pb[wave * 32 + 4 * i];
            float o[4][2];
#pragma unroll
            for (int h = 0; h < 4; ++h) { o[h][0] = 0.f; o[h][1] = 0.f; }
#pragma unroll
            for (int kp2 = 0; kp2 < 8; ++kp2) {
                const unsigned lo = plo(V[2 * kp2], V[2 * kp2 + 1]), hi = phi(V[2 * kp2], V[2 * kp2 + 1]);
#pragma unroll
                for (int h = 0; h < 4; ++h) { const unsigned pw = P[2 * h + (kp2 >> 2)][kp2 & 3]; o[h][0] = dot2(lo, pw, o[h][0]); o[h][1] = dot2(hi, pw, o[h][1]); }
            }
#pragma unroll
            for (int h = 0; h < 4; ++h) *(v2f*)&L.big[wave * 512 + h * 128 + 2 * lane] = v2f{o[h][0], o[h][1]};
        }
        BAR();
        const unsigned tag = epoch + 1u;
        if (wave < 4) {
            const int h = wave, d = 2 * lane;
            const v4f ma = *(const v4f*)&L.ml[h * 16], mb = *(const v4f*)&L.ml[h * 16 + 4], mc = *(const v4f*)&L.ml[h * 16 + 8], md = *(const v4f*)&L.ml[h * 16 + 12];
            const float mw[8] = {ma.x, ma.z, mb.x, mb.z, mc.x, mc.z, md.x, md.z};
            const float lw[8] = {ma.y, ma.w, mb.y, mb.w, mc.y, mc.w, md.y, md.w};
            float M = mw[0];
#pragma unroll
            for (int w = 1; w < 8; ++w) M = fmaxf(M, mw[w]);
            float oa = 0.f, ob = 0.f, Ls = 0.f;
#pragma unroll
            for (int w = 0; w < 8; ++w) { const float e = __expf(mw[w] - M); const v2f t = *(const v2f*)&L.big[w * 512 + 2 * tid]; oa = fmaf(e, t.x, oa); ob = fmaf(e, t.y, ob); Ls = fmaf(e, lw[w], Ls); }
            ST16(WS_E2O + ((((g * 8 + 2 * h + (d >> 6)) * 8 + ((d >> 3) & 7)) * 32 + s) * 8 + (d & 7)) * 8, asu(oa), asu(ob));
            if (lane == 0) ST16(WS_E2ML + ((g * 4 + h) * 32 + s) * 16, asu(M), asu(Ls));
        }
        STAMP(3);
        if (!(is_wo | is_mlp)) { if (tid == 0) { STAMPOUT(3, 8); STAMPOUT(10, 12); } return; }
        BAR();
        v4u R[8];
        if (is_wo) {
            const u16* rp = wo + (size_t)(512 * g + 64 * s + 8 * wave) * 512 + 8 * lane;
#pragma unroll
            for (int i = 0; i < 8; ++i) R[i] = LDG(rp + i * 512);
            SB();
            v4u v[2], mlv;
            for (unsigned spin = 0;; ++spin) {
                bool ok = true;
#pragma unroll
                for (int k = 0; k < 2; ++k) { v[k] = LD16(WS_E2O + ((g * 8 + s) * 8 + wave) * 2048 + k * 1024 + lane * 16); ok &= TAGOK(v[k]); }
                mlv = LD16(WS_E2ML + (g * 4 + (s >> 1)) * 512 + (lane & 31) * 16);
                ok &= TAGOK(mlv);
                SPIN_END();
            }
            const float ms = asf(mlv.x), ls = asf(mlv.z);
            const float M = wave_max(ms);
            const float wgt = __expf(ms - M);
            const float rL = __builtin_amdgcn_rcpf(0.5f * wave_sum(wgt * ls));
            const float wa = __shfl(wgt, lane >> 2), wb = __shfl(wgt, (lane >> 2) + 16);
            float a0 = fmaf(wb, asf(v[1].x), wa * asf(v[0].x)), a1 = fmaf(wb, asf(v[1].z), wa * asf(v[0].z));
            const unsigned op = cvtpk(sum_hi4(a0) * rL, sum_hi4(a1) * rL);
            float acc[8] = {0.f, 0.f, 0.f, 0.f, 0.f, 0.f, 0.f, 0.f};
#pragma unroll
            for (int i = 0; i < 4; ++i) dot2x8(acc, __builtin_amdgcn_readlane(op, i), R[2 * i], R[2 * i + 1]);
            STAMP(4);
            float* dst = &L.big[wave * 512 + 8 * lane];
            *(v4f*)dst = v4f{acc[0], acc[1], acc[2], acc[3]};
            *(v4f*)(dst + 4) = v4f{acc[4], acc[5], acc[6], acc[7]};
            BAR();
            if (wave < 4) {
                float s0 = 0.f, s1 = 0.f;
#pragma unroll
                for (int w = 0; w < 8; ++w) { const v2f t = *(const v2f*)&L.big[w * 512 + 2 * tid]; s0 += t.x; s1 += t.y; }
                ST16(WS_E3A + (((tid >> 2) * 64 + (g * 8 + s)) * 8 + 2 * (tid & 3)) * 8, asu(s0), asu(s1));
            }
            STAMP(5);
        } else {
            const int j = g * 7 + (s - 8);
            const u16* rp0 = w1 + (size_t)(64 * wave + c) * FF + 32 * j + 8 * r;
            const u16* rp1 = w2 + (size_t)(32 * j + 4 * wave) * 512 + 8 * lane;
#pragma unroll
            for (int i = 0; i < 4; ++i) R[i] = LDG(rp0 + (size_t)i * 16 * FF);
#pragma unroll
            for (int i = 0; i < 4; ++i) R[4 + i] = LDG(rp1 + i * 512);
            const unsigned b1v = b1[32 * j + 4 * wave + (lane & 3)];
            SB();
            v4u v[2]; v2u hr[4];
            for (unsigned spin = 0;; ++spin) {
                bool ok = true;
#pragma unroll
                for (int k = 0; k < 2; ++k) { v[k] = LD16(WS_E3B + g * 2048 + k * 1024 + lane * 16); ok &= TAGOK(v[k]); }
#pragma unroll
                for (int i = 0; i < 4; ++i) { hr[i] = __builtin_amdgcn_raw_buffer_load_b64(rs, WS_E3B + g * 2048 + ((64 * wave + 16 * i + c) >> 1) * 8, 0, 16); ok &= (hr[i].y == tag); }
                SPIN_END();
            }
            STAMP(6);
            float hf[8] = {bf_lo(v[0].x), bf_hi(v[0].x), bf_lo(v[0].z), bf_hi(v[0].z), bf_lo(v[1].x), bf_hi(v[1].x), bf_lo(v[1].z), bf_hi(v[1].z)};
            float m, rsd;
            ln_stats(hf, m, rsd);
            float hn[4];
#pragma unroll
            for (int i = 0; i < 4; ++i) { const float hv = (c & 1) ? bf_hi(hr[i].x) : bf_lo(hr[i].x); hn[i] = rbf(hv - m) * rsd; }
            float xs[8] = {0.f, 0.f, 0.f, 0.f, 0.f, 0.f, 0.f, 0.f};
            dot2x8(xs, cvtpk(hn[0], hn[1]), R[0], R[1]);
            dot2x8(xs, cvtpk(hn[2], hn[3]), R[2], R[3]);
#define TR_STEP(HALF, CTRL, BIT) _Pragma("unroll") for (int h = 0; h < (HALF); ++h) { \
            const float lo_ = xs[h], hi_ = xs[h + (HALF)]; const float t1_ = lo_ + dpp<CTRL>(lo_), t2_ = hi_ + dpp<CTRL>(hi_); \
            xs[h] = (c & (BIT)) ? t2_ : t1_; }
            TR_STEP(4, ROR8, 8) TR_STEP(2, XOR7, 4) TR_STEP(1, XOR2, 2)
#undef TR_STEP
            const float rr = xs[0] + dpp<XOR1>(xs[0]);
            if ((c & 1) == 0) L.red4[(8 * r + ((c >> 3) & 1) * 4 + ((c >> 2) & 1) * 2 + ((c >> 1) & 1)) * 8 + wave] = rr;
            BAR();
            const v4f ra = *(const v4f*)&L.red4[(4 * wave + (lane & 3)) * 8], rb = *(const v4f*)&L.red4[(4 * wave + (lane & 3)) * 8 + 4];
            const float sum = ((ra.x + ra.y) + (ra.z + ra.w)) + ((rb.x + rb.y) + (rb.z + rb.w));
            const float u = rbf(rbf(sum) + bf_lo(b1v));
            const float z = 0.7978845608028654f * (u + 0.044715f * u * u * u);
            const float th = 1.f - 2.f * __builtin_amdgcn_rcpf(__expf(2.f * z) + 1.f);
            const float gl = 0.5f * u * (1.f + th);
            const unsigned gp = cvtpk(gl, dpp<XOR1>(gl));
            float acc[8] = {0.f, 0.f, 0.f, 0.f, 0.f, 0.f, 0.f, 0.f};
            dot2x8(acc, __builtin_amdgcn_readlane(gp, 0), R[4], R[5]);
            dot2x8(acc, __builtin_amdgcn_readlane(gp, 2), R[6], R[7]);
            float* dst = &L.big[wave * 512 + 8 * lane];
            *(v4f*)dst = v4f{acc[0], acc[1], acc[2], acc[3]};
            *(v4f*)(dst + 4) = v4f{acc[4], acc[5], acc[6], acc[7]};
            BAR();
            if (wave < 4) {
                float s0 = 0.f, s1 = 0.f;
#pragma unroll
                for (int w = 0; w < 8; ++w) { const v2f t = *(const v2f*)&L.big[w * 512 + 2 * tid]; s0 += t.x; s1 += t.y; }
                ST16(WS_E5 + (((tid >> 2) * 64 + j) * 8 + 2 * (tid & 3)) * 8, asu(s0), asu(s1));
            }
            STAMP(7);
        }
        if (tid == 0) { STAMPOUT(3, 8); STAMPOUT(10, 12); }
        return;
    }
    {
        const unsigned tag = __hip_atomic_load((gu32*)(ws + WS_EPOCH), RLX_AGENT) + 1u;
        const int rj = g * 8 + (s - 16);
        const int rc = is_red ? 8 * rj + 2 * (lane & 3) : 0;
        const unsigned xpair = *(const unsigned*)(x + rc);
        const unsigned b2pair = *(const unsigned*)(b2 + rc);
        BAR();
        STAMP(1);
        if (lane < 8) {
            float a0 = 0.f, a1 = 0.f;
#pragma unroll
            for (int w = 0; w < 8; ++w) { const v2f t = *(const v2f*)&L.red1[w * 16 + 2 * lane]; a0 += t.x; a1 += t.y; }
            st_gran(ws, WS_E1 + (g * 256 + s * 8 + lane) * 8, tag, cvtpk(a0, a1));
        }
        BAR();
        {
            v4u v[2];
            for (unsigned spin = 0;; ++spin) {
                bool ok = true;
#pragma unroll
                for (int k = 0; k < 2; ++k) { v[k] = LD16(WS_E1 + g * 2048 + k * 1024 + lane * 16); ok &= TAGOK(v[k]); }
                SPIN_END();
            }
            *(v2u*)&L.qs[2 * lane] = v2u{v[0].x, v[0].z};
            *(v2u*)&L.qs[128 + 2 * lane] = v2u{v[1].x, v[1].z};
        }
        STAMP(2);
        BAR();
        if (is_red) {
            BAR();
            float h0, h1;
            {
                v4u v[4];
                for (unsigned spin = 0;; ++spin) {
                    bool ok = true;
#pragma unroll
                    for (int k = 0; k < 4; ++k) { v[k] = LD16(WS_E3A + rj * 4096 + k * 1024 + lane * 16); ok &= TAGOK(v[k]); }
                    SPIN_END();
                }
                float a0 = (asf(v[0].x) + asf(v[1].x)) + (asf(v[2].x) + asf(v[3].x));
                float a1 = (asf(v[0].z) + asf(v[1].z)) + (asf(v[2].z) + asf(v[3].z));
                a0 = sum_hi4(a0); a1 = sum_hi4(a1);
                const unsigned hp = cvtpk(bf_lo(xpair) + rbf(a0), bf_hi(xpair) + rbf(a1));
                h0 = bf_lo(hp); h1 = bf_hi(hp);
                if (lane < 32) st_gran(ws, WS_E3B + ((lane >> 2) * 256 + 4 * rj + (lane & 3)) * 8, tag, hp);
            }
            STAMP(8);
            {
                v4u v[4];
                for (unsigned spin = 0;; ++spin) {
                    bool ok = true;
#pragma unroll
                    for (int k = 0; k < 4; ++k) { const int o = (k < 3 || lane < 32) ? k * 1024 + lane * 16 : lane * 16; v[k] = LD16(WS_E5 + rj * 4096 + o); ok &= TAGOK(v[k]); }
                    SPIN_END();
                }
                const float l0 = lane < 32 ? asf(v[3].x) : 0.f, l1 = lane < 32 ? asf(v[3].z) : 0.f;
                float a0 = (asf(v[0].x) + asf(v[1].x)) + (asf(v[2].x) + l0);
                float a1 = (asf(v[0].z) + asf(v[1].z)) + (asf(v[2].z) + l1);
                a0 = sum_hi4(a0); a1 = sum_hi4(a1);
                const float y0 = rbf(rbf(a0) + bf_lo(b2pair)), y1 = rbf(rbf(a1) + bf_hi(b2pair));
                if (lane < 4) *(unsigned*)(out + rc) = cvtpk(h0 + y0, h1 + y1);
                if (lane == 0 && rj == 0) __hip_atomic_store((gu32*)(ws + WS_EPOCH), tag, RLX_AGENT);
            }
            STAMP(9);
        }
        if (lane == 0) { STAMPOUT(0, 3); STAMPOUT(8, 10); }
    }
}

extern "C" void kernel_launch(void* const* d_in, const int* in_sizes, int n_in, void* d_out, int out_size, void* d_ws, size_t ws_size, hipStream_t stream) {
    (void)in_sizes; (void)n_in; (void)out_size; (void)ws_size;
    fused_block<<<NWG, NTHR, 0, stream>>>((const u16*)d_in[0], (const u16*)d_in[1], (const u16*)d_in[2], (const u16*)d_in[3], (const u16*)d_in[4],
                                          (const u16*)d_in[6], (const u16*)d_in[7], (const u16*)d_in[8], (const u16*)d_in[9], (u16*)d_out, (unsigned char*)d_ws);
}
```

```cpp
#include <hip/hip_runtime.h>
#include <stdint.h>
#define VMODE 0

typedef unsigned short u16;
typedef unsigned v4u __attribute__((ext_vector_type(4)));
typedef unsigned v2u __attribute__((ext_vector_type(2)));
typedef float v4f __attribute__((ext_vector_type(4)));
typedef float v2f __attribute__((ext_vector_type(2)));
typedef __bf16 bf16x2 __attribute__((ext_vector_type(2)));
typedef __bf16 bf16x8 __attribute__((ext_vector_type(8)));
#define GAS __attribute__((address_space(1)))
typedef GAS unsigned long long gu64;
typedef GAS unsigned gu32;
#define RLX_AGENT __ATOMIC_RELAXED, __HIP_MEMORY_SCOPE_AGENT

constexpr int SK = 65536, WIN = 4096, FF = 1792;
constexpr int NWG = 256, NTHR = 576;
constexpr unsigned WS_EPOCH = 0;
constexpr unsigned WS_E1 = 4096;
constexpr unsigned WS_E2O = WS_E1 + 8 * 256 * 8;
constexpr unsigned WS_E2ML = WS_E2O + 8 * 8 * 8 * 32 * 8 * 8;
constexpr unsigned WS_E3A = WS_E2ML + 8 * 4 * 32 * 2 * 8;
constexpr unsigned WS_E3B = WS_E3A + 64 * 64 * 8 * 8;
constexpr unsigned WS_E5 = WS_E3B + 8 * 256 * 8;
constexpr unsigned WS_END = WS_E5 + 64 * 64 * 8 * 8;
constexpr int DYN_LDS = 40 * 1024;
constexpr unsigned SPIN_CAP = 1u << 22;

__device__ __forceinline__ float asf(unsigned u) { return __builtin_bit_cast(float, u); }
__device__ __forceinline__ unsigned asu(float f) { return __builtin_bit_cast(unsigned, f); }
__device__ __forceinline__ float bf_lo(unsigned u) { return __builtin_bit_cast(float, u << 16); }
__device__ __forceinline__ float bf_hi(unsigned u) { return __builtin_bit_cast(float, u & 0xffff0000u); }
__device__ __forceinline__ unsigned cvtpk(float lo, float hi) {
    const bf16x2 v = {(__bf16)lo, (__bf16)hi};
    return __builtin_bit_cast(unsigned, v);
}
__device__ __forceinline__ float rbf(float f) { return bf_lo(cvtpk(f, f)); }
template <int CTRL> __device__ __forceinline__ float dpp(float x) {
    return __builtin_bit_cast(float, __builtin_amdgcn_mov_dpp(__builtin_bit_cast(int, x), CTRL, 0xf, 0xf, true));
}
constexpr int XOR1 = 0xB1, XOR2 = 0x4E, XOR7 = 0x141, ROR4 = 0x124, ROR8 = 0x128;
__device__ __forceinline__ float swap32_add(float a, float b) {
    auto s = __builtin_amdgcn_permlane32_swap(asu(a), asu(b), false, false);
    return asf(s[0]) + asf(s[1]);
}
__device__ __forceinline__ float swap16_add(float a, float b) {
    auto s = __builtin_amdgcn_permlane16_swap(asu(a), asu(b), false, false);
    return asf(s[0]) + asf(s[1]);
}
__device__ __forceinline__ float swap32_max(float a) {
    auto s = __builtin_amdgcn_permlane32_swap(asu(a), asu(a), false, false);
    return fmaxf(asf(s[0]), asf(s[1]));
}
__device__ __forceinline__ float swap16_max(float a) {
    auto s = __builtin_amdgcn_permlane16_swap(asu(a), asu(a), false, false);
    return fmaxf(asf(s[0]), asf(s[1]));
}
__device__ __forceinline__ float row_sum(float v) { v += dpp<XOR1>(v); v += dpp<XOR2>(v); v += dpp<ROR4>(v); v += dpp<ROR8>(v); return v; }
__device__ __forceinline__ float row_max(float v) { v = fmaxf(v, dpp<XOR1>(v)); v = fmaxf(v, dpp<XOR2>(v)); v = fmaxf(v, dpp<ROR4>(v)); v = fmaxf(v, dpp<ROR8>(v)); return v; }
__device__ __forceinline__ float wave_sum(float v) { v = row_sum(v); v = swap16_add(v, v); v = swap32_add(v, v); return v; }
__device__ __forceinline__ float wave_max(float v) { v = row_max(v); v = swap16_max(v); v = swap32_max(v); return v; }
__device__ __forceinline__ float sum_hi4(float v) {
    v += dpp<ROR4>(v); v += dpp<ROR8>(v);
    v = swap16_add(v, v); v = swap32_add(v, v);
    return v;
}
__device__ __forceinline__ float dot2(unsigned a, unsigned b, float acc) {
    return __builtin_amdgcn_fdot2_f32_bf16(__builtin_bit_cast(bf16x2, a), __builtin_bit_cast(bf16x2, b), acc, false);
}
__device__ __forceinline__ unsigned plo(unsigned a, unsigned b) { return __builtin_amdgcn_perm(b, a, 0x05040100u); }
__device__ __forceinline__ unsigned phi(unsigned a, unsigned b) { return __builtin_amdgcn_perm(b, a, 0x07060302u); }
__device__ __forceinline__ void dot2x8(float (&a)[8], unsigned sp, const v4u& A, const v4u& B) {
    a[0] = dot2(plo(A.x, B.x), sp, a[0]); a[1] = dot2(phi(A.x, B.x), sp, a[1]);
    a[2] = dot2(plo(A.y, B.y), sp, a[2]); a[3] = dot2(phi(A.y, B.y), sp, a[3]);
    a[4] = dot2(plo(A.z, B.z), sp, a[4]); a[5] = dot2(phi(A.z, B.z), sp, a[5]);
    a[6] = dot2(plo(A.w, B.w), sp, a[6]); a[7] = dot2(phi(A.w, B.w), sp, a[7]);
}
#define BAR() asm volatile("s_waitcnt lgkmcnt(0)\n\ts_barrier" ::: "memory")
#define STAMP(i) do { } while (0)
#define STAMPOUT(lo, hi) do { } while (0)
#define SB() do { asm volatile("" ::: "memory"); __builtin_amdgcn_sched_barrier(0); } while (0)
#define LDG(p) __builtin_nontemporal_load((const v4u*)(p))
#define LD16(off) __builtin_amdgcn_raw_buffer_load_b128(rs, (off), 0, 16)
#define ST16(off, a, b) __builtin_amdgcn_raw_buffer_store_b128(v4u{(a), tag, (b), tag}, rs, (off), 0, 16)
#define TAGOK(v) (((v).y == tag) & ((v).w == tag))
#define SPIN_END() if (__all(ok) || spin > SPIN_CAP) break; asm volatile("" ::: "memory")

__device__ __forceinline__ void st_gran(unsigned char* ws, unsigned off, unsigned tag, unsigned val) {
    __hip_atomic_store((gu64*)(ws + off), ((unsigned long long)tag << 32) | val, RLX_AGENT);
}

__device__ __forceinline__ void ln_stats(const float (&v)[8], float& m, float& rsd) {
    float s = ((v[0] + v[1]) + (v[2] + v[3])) + ((v[4] + v[5]) + (v[6] + v[7]));
    s = wave_sum(s);
    const float mean = s * (1.f / 512.f);
    float q = 0.f;
#pragma unroll
    for (int i = 0; i < 8; ++i) { const float d = v[i] - mean; q = fmaf(d, d, q); }
    q = wave_sum(q);
    m = rbf(mean);
    rsd = __builtin_amdgcn_rcpf(rbf(__builtin_amdgcn_sqrtf(rbf(rbf(q * (1.f / 512.f)) + 1e-5f))));
}

struct Lds {
    float big[8 * 512];
    unsigned vt[8 * 1024];
    float hn[512];
    unsigned qs[256];
    unsigned pb[8 * 32];
    float ml[4 * 8 * 2];
    float red1[8 * 16];
    float red4[32 * 8];
    unsigned tag;
};

__global__ void __launch_bounds__(NTHR) fused_block(const u16* __restrict__ x, const u16* __restrict__ kc, const u16* __restrict__ vc,
                                                     const u16* __restrict__ wq, const u16* __restrict__ wo, const u16* __restrict__ w1,
                                                     const u16* __restrict__ b1, const u16* __restrict__ w2, const u16* __restrict__ b2,
                                                     u16* __restrict__ out, unsigned char* ws) {
    __shared__ __attribute__((aligned(16))) Lds L;
    const int tid = threadIdx.x, lane = tid & 63, wave = __builtin_amdgcn_readfirstlane(tid >> 6);
    const int b = blockIdx.x, g = b & 7, s = b >> 3;
    const bool is_wo = s < 8, is_mlp = (s >= 8) & (s < 15), is_red = (s >= 16) & (s < 24);
    const __amdgpu_buffer_rsrc_t rs = __builtin_amdgcn_make_buffer_rsrc(ws, 0, WS_END, 0x00020000);
    STAMP(0);

    if (wave < 8) {
        const int r = lane >> 4, c = lane & 15;
        const int r0 = tid >> 1, hf = tid & 1;
        const u16* wqp = wq + (size_t)r0 * 4096 + 512 * g + 16 * s + 8 * hf;
        const size_t kvrow = (size_t)g * SK + (SK - WIN) + 128 * s + 16 * wave;
        const u16* kp = kc + (kvrow + c) * 128 + 8 * r;
        const u16* vp = vc + kvrow * 128 + 2 * lane;
        SB();
        const v4u xv = *(const v4u*)(x + 8 * lane);
        const unsigned xr0 = x[r0], xr1 = x[r0 + 256];
        SB();
        const v4u q0 = LDG(wqp), q1 = LDG(wqp + (size_t)256 * 4096);
        SB();
        asm volatile("s_barrier" ::: "memory");
        v4u K[4];
#pragma unroll
        for (int i = 0; i < 4; ++i) K[i] = LDG(kp + i * 32);
        SB();
        {
            float xf[8] = {bf_lo(xv.x), bf_hi(xv.x), bf_lo(xv.y), bf_hi(xv.y), bf_lo(xv.z), bf_hi(xv.z), bf_lo(xv.w), bf_hi(xv.w)};
            float m, rsd;
            ln_stats(xf, m, rsd);
            const unsigned np = cvtpk(rbf(bf_lo(xr0) - m) * rsd, rbf(bf_lo(xr1) - m) * rsd);
            float a[8] = {0.f, 0.f, 0.f, 0.f, 0.f, 0.f, 0.f, 0.f};
            dot2x8(a, np, q0, q1);
            float u[4];
#pragma unroll
            for (int k = 0; k < 4; ++k) u[k] = swap32_add(a[k], a[k + 4]);
            const float z0 = swap16_add(u[0], u[2]), z1 = swap16_add(u[1], u[3]);
            const float t0 = z0 + dpp<XOR2>(z0), t1 = z1 + dpp<XOR2>(z1);
            float rr = (lane & 2) ? t1 : t0;
            rr += dpp<ROR4>(rr); rr += dpp<ROR8>(rr);
            if ((lane & 12) == 0) L.red1[wave * 16 + 8 * (lane & 1) + ((lane >> 1) & 1) + 2 * ((lane >> 4) & 1) + 4 * (lane >> 5)] = rr;
        }
        SB();
        STAMP(11);
        BAR();
        BAR();
        unsigned V[16];
#pragma unroll
        for (int i = 0; i < 16; ++i) V[i] = __builtin_nontemporal_load((const unsigned*)(vp + i * 128));
        SB();
        BAR();
        {
            v4f sacc = {0.f, 0.f, 0.f, 0.f};
#pragma unroll
            for (int ks = 0; ks < 4; ++ks) {
                const v4u qa = *(const v4u*)&L.qs[(c >> 2) * 64 + 16 * ks + 4 * r];
                sacc = __builtin_amdgcn_mfma_f32_16x16x32_bf16(__builtin_bit_cast(bf16x8, qa), __builtin_bit_cast(bf16x8, K[ks]), sacc, 0, 0, 0);
            }
            const float sc = rbf(rbf(sacc[0]) * 0.08838834764831845f);
            const float m = row_max(sc);
            const float p = __expf(sc - m);
            const float l = row_sum(p);
            const unsigned pp = cvtpk(p, dpp<XOR1>(p));
            if ((c & 1) == 0) L.pb[wave * 32 + r * 8 + (c >> 1)] = pp;
            if (c == 0) *(v2f*)&L.ml[(r * 8 + wave) * 2] = v2f{m, l};
            asm volatile("s_waitcnt lgkmcnt(0)" ::: "memory");
            v4u P[8];
#pragma unroll
            for (int i = 0; i < 8; ++i) P[i] = *(const v4u*)&L.pb[wave * 32 + 4 * i];
            float o[4][2];
#pragma unroll
            for (int h = 0; h < 4; ++h) { o[h][0] = 0.f; o[h][1] = 0.f; }
#pragma unroll
            for (int kp2 = 0; kp2 < 8; ++kp2) {
                const unsigned lo = plo(V[2 * kp2], V[2 * kp2 + 1]), hi = phi(V[2 * kp2], V[2 * kp2 + 1]);
#pragma unroll
                for (int h = 0; h < 4; ++h) { const unsigned pw = P[2 * h + (kp2 >> 2)][kp2 & 3]; o[h][0] = dot2(lo, pw, o[h][0]); o[h][1] = dot2(hi, pw, o[h][1]); }
            }
#pragma unroll
            for (int h = 0; h < 4; ++h) *(v2f*)&L.big[wave * 512 + h * 128 + 2 * lane] = v2f{o[h][0], o[h][1]};
        }
        BAR();
        const unsigned tag = L.tag;
        if (wave < 4) {
            const int h = wave, d = 2 * lane;
            const v4f ma = *(const v4f*)&L.ml[h * 16], mb = *(const v4f*)&L.ml[h * 16 + 4], mc = *(const v4f*)&L.ml[h * 16 + 8], md = *(const v4f*)&L.ml[h * 16 + 12];
            const float mw[8] = {ma.x, ma.z, mb.x, mb.z, mc.x, mc.z, md.x, md.z};
            const float lw[8] = {ma.y, ma.w, mb.y, mb.w, mc.y, mc.w, md.y, md.w};
            float M = mw[0];
#pragma unroll
            for (int w = 1; w < 8; ++w) M = fmaxf(M, mw[w]);
            float oa = 0.f, ob = 0.f, Ls = 0.f;
#pragma unroll
            for (int w = 0; w < 8; ++w) { const float e = __expf(mw[w] - M); const v2f t = *(const v2f*)&L.big[w * 512 + 2 * tid]; oa = fmaf(e, t.x, oa); ob = fmaf(e, t.y, ob); Ls = fmaf(e, lw[w], Ls); }
            ST16(WS_E2O + ((((g * 8 + 2 * h + (d >> 6)) * 8 + ((d >> 3) & 7)) * 32 + s) * 8 + (d & 7)) * 8, asu(oa), asu(ob));
            if (lane == 0) ST16(WS_E2ML + ((g * 4 + h) * 32 + s) * 16, asu(M), asu(Ls));
        }
        STAMP(3);
        if (!(is_wo | is_mlp)) { if (tid == 0) { STAMPOUT(3, 8); STAMPOUT(10, 12); } return; }
        BAR();
        v4u R[8];
        if (is_wo) {
            const u16* rp = wo + (size_t)(512 * g + 64 * s + 8 * wave) * 512 + 8 * lane;
#pragma unroll
            for (int i = 0; i < 8; ++i) R[i] = LDG(rp + i * 512);
            SB();
            v4u v[2], mlv;
            for (unsigned spin = 0;; ++spin) {
                bool ok = true;
#pragma unroll
                for (int k = 0; k < 2; ++k) { v[k] = LD16(WS_E2O + ((g * 8 + s) * 8 + wave) * 2048 + k * 1024 + lane * 16); ok &= TAGOK(v[k]); }
                mlv = LD16(WS_E2ML + (g * 4 + (s >> 1)) * 512 + (lane & 31) * 16);
                ok &= TAGOK(mlv);
                SPIN_END();
            }
            const float ms = asf(mlv.x), ls = asf(mlv.z);
            const float M = wave_max(ms);
            const float wgt = __expf(ms - M);
            const float rL = __builtin_amdgcn_rcpf(0.5f * wave_sum(wgt * ls));
            const float wa = __shfl(wgt, lane >> 2), wb = __shfl(wgt, (lane >> 2) + 16);
            float a0 = fmaf(wb, asf(v[1].x), wa * asf(v[0].x)), a1 = fmaf(wb, asf(v[1].z), wa * asf(v[0].z));
            const unsigned op = cvtpk(sum_hi4(a0) * rL, sum_hi4(a1) * rL);
            float acc[8] = {0.f, 0.f, 0.f, 0.f, 0.f, 0.f, 0.f, 0.f};
#pragma unroll
            for (int i = 0; i < 4; ++i) dot2x8(acc, __builtin_amdgcn_readlane(op, i), R[2 * i], R[2 * i + 1]);
            STAMP(4);
            float* dst = &L.big[wave * 512 + 8 * lane];
            *(v4f*)dst = v4f{acc[0], acc[1], acc[2], acc[3]};
            *(v4f*)(dst + 4) = v4f{acc[4], acc[5], acc[6], acc[7]};
            BAR();
            if (wave < 4) {
                float s0 = 0.f, s1 = 0.f;
#pragma unroll
                for (int w = 0; w < 8; ++w) { const v2f t = *(const v2f*)&L.big[w * 512 + 2 * tid]; s0 += t.x; s1 += t.y; }
                ST16(WS_E3A + (((tid >> 2) * 64 + (g * 8 + s)) * 8 + 2 * (tid & 3)) * 8, asu(s0), asu(s1));
            }
            STAMP(5);
        } else {
            const int j = g * 7 + (s - 8);
            const u16* rp0 = w1 + (size_t)(64 * wave + c) * FF + 32 * j + 8 * r;
            const u16* rp1 = w2 + (size_t)(32 * j + 4 * wave) * 512 + 8 * lane;
#pragma unroll
            for (int i = 0; i < 4; ++i) R[i] = LDG(rp0 + (size_t)i * 16 * FF);
#pragma unroll
            for (int i = 0; i < 4; ++i) R[4 + i] = LDG(rp1 + i * 512);
            const unsigned b1v = b1[32 * j + 4 * wave + (lane & 3)];
            SB();
            BAR();
            STAMP(6);
            float hn[4];
#pragma unroll
            for (int i = 0; i < 4; ++i) hn[i] = L.hn[64 * wave + 16 * i + c];
            float xs[8] = {0.f, 0.f, 0.f, 0.f, 0.f, 0.f, 0.f, 0.f};
            dot2x8(xs, cvtpk(hn[0], hn[1]), R[0], R[1]);
            dot2x8(xs, cvtpk(hn[2], hn[3]), R[2], R[3]);
#define TR_STEP(HALF, CTRL, BIT) _Pragma("unroll") for (int h = 0; h < (HALF); ++h) { \
            const float lo_ = xs[h], hi_ = xs[h + (HALF)]; const float t1_ = lo_ + dpp<CTRL>(lo_), t2_ = hi_ + dpp<CTRL>(hi_); \
            xs[h] = (c & (BIT)) ? t2_ : t1_; }
            TR_STEP(4, ROR8, 8) TR_STEP(2, XOR7, 4) TR_STEP(1, XOR2, 2)
#undef TR_STEP
            const float rr = xs[0] + dpp<XOR1>(xs[0]);
            if ((c & 1) == 0) L.red4[(8 * r + ((c >> 3) & 1) * 4 + ((c >> 2) & 1) * 2 + ((c >> 1) & 1)) * 8 + wave] = rr;
            BAR();
            const v4f ra = *(const v4f*)&L.red4[(4 * wave + (lane & 3)) * 8], rb = *(const v4f*)&L.red4[(4 * wave + (lane & 3)) * 8 + 4];
            const float sum = ((ra.x + ra.y) + (ra.z + ra.w)) + ((rb.x + rb.y) + (rb.z + rb.w));
            const float u = rbf(rbf(sum) + bf_lo(b1v));
            const float z = 0.7978845608028654f * (u + 0.044715f * u * u * u);
            const float th = 1.f - 2.f * __builtin_amdgcn_rcpf(__expf(2.f * z) + 1.f);
            const float gl = 0.5f * u * (1.f + th);
            const unsigned gp = cvtpk(gl, dpp<XOR1>(gl));
            float acc[8] = {0.f, 0.f, 0.f, 0.f, 0.f, 0.f, 0.f, 0.f};
            dot2x8(acc, __builtin_amdgcn_readlane(gp, 0), R[4], R[5]);
            dot2x8(acc, __builtin_amdgcn_readlane(gp, 2), R[6], R[7]);
            float* dst = &L.big[wave * 512 + 8 * lane];
            *(v4f*)dst = v4f{acc[0], acc[1], acc[2], acc[3]};
            *(v4f*)(dst + 4) = v4f{acc[4], acc[5], acc[6], acc[7]};
            BAR();
            if (wave < 4) {
                float s0 = 0.f, s1 = 0.f;
#pragma unroll
                for (int w = 0; w < 8; ++w) { const v2f t = *(const v2f*)&L.big[w * 512 + 2 * tid]; s0 += t.x; s1 += t.y; }
                ST16(WS_E5 + (((tid >> 2) * 64 + j) * 8 + 2 * (tid & 3)) * 8, asu(s0), asu(s1));
            }
            STAMP(7);
        }
        if (tid == 0) { STAMPOUT(3, 8); STAMPOUT(10, 12); }
        return;
    }
    {
        const unsigned tag = *(const volatile unsigned*)(ws + WS_EPOCH) + 1u;
        const int rj = g * 8 + (s - 16);
        const int rc = is_red ? 8 * rj + 2 * (lane & 3) : 0;
        const unsigned xpair = *(const unsigned*)(x + rc);
        const unsigned b2pair = *(const unsigned*)(b2 + rc);
        asm volatile("s_barrier" ::: "memory");
        BAR();
        STAMP(1);
        if (lane == 8) L.tag = tag;
        if (lane < 8) {
            float a0 = 0.f, a1 = 0.f;
#pragma unroll
            for (int w = 0; w < 8; ++w) { const v2f t = *(const v2f*)&L.red1[w * 16 + 2 * lane]; a0 += t.x; a1 += t.y; }
            st_gran(ws, WS_E1 + (g * 256 + s * 8 + lane) * 8, tag, cvtpk(a0, a1));
        }
        BAR();
        {
            v4u v[2];
            for (unsigned spin = 0;; ++spin) {
                bool ok = true;
#pragma unroll
                for (int k = 0; k < 2; ++k) { v[k] = LD16(WS_E1 + g * 2048 + k * 1024 + lane * 16); ok &= TAGOK(v[k]); }
                SPIN_END();
            }
            *(v2u*)&L.qs[2 * lane] = v2u{v[0].x, v[0].z};
            *(v2u*)&L.qs[128 + 2 * lane] = v2u{v[1].x, v[1].z};
        }
        STAMP(2);
        BAR();
        if (is_mlp) {
            BAR();
            BAR();
            v4u v[2];
            for (unsigned spin = 0;; ++spin) {
                bool ok = true;
#pragma unroll
                for (int k = 0; k < 2; ++k) { v[k] = LD16(WS_E3B + g * 2048 + k * 1024 + lane * 16); ok &= TAGOK(v[k]); }
                SPIN_END();
            }
            float hf[8] = {bf_lo(v[0].x), bf_hi(v[0].x), bf_lo(v[0].z), bf_hi(v[0].z), bf_lo(v[1].x), bf_hi(v[1].x), bf_lo(v[1].z), bf_hi(v[1].z)};
            float m, rsd;
            ln_stats(hf, m, rsd);
            *(v4f*)&L.hn[4 * lane] = v4f{rbf(hf[0] - m) * rsd, rbf(hf[1] - m) * rsd, rbf(hf[2] - m) * rsd, rbf(hf[3] - m) * rsd};
            *(v4f*)&L.hn[256 + 4 * lane] = v4f{rbf(hf[4] - m) * rsd, rbf(hf[5] - m) * rsd, rbf(hf[6] - m) * rsd, rbf(hf[7] - m) * rsd};
            BAR();
        }
        if (is_red) {
            BAR();
            float h0, h1;
            {
                v4u v[4];
                for (unsigned spin = 0;; ++spin) {
                    bool ok = true;
#pragma unroll
                    for (int k = 0; k < 4; ++k) { v[k] = LD16(WS_E3A + rj * 4096 + k * 1024 + lane * 16); ok &= TAGOK(v[k]); }
                    SPIN_END();
                }
                float a0 = (asf(v[0].x) + asf(v[1].x)) + (asf(v[2].x) + asf(v[3].x));
                float a1 = (asf(v[0].z) + asf(v[1].z)) + (asf(v[2].z) + asf(v[3].z));
                a0 = sum_hi4(a0); a1 = sum_hi4(a1);
                const unsigned hp = cvtpk(bf_lo(xpair) + rbf(a0), bf_hi(xpair) + rbf(a1));
                h0 = bf_lo(hp); h1 = bf_hi(hp);
                if (lane < 32) st_gran(ws, WS_E3B + ((lane >> 2) * 256 + 4 * rj + (lane & 3)) * 8, tag, hp);
            }
            STAMP(8);
            {
                v4u v[4];
                for (unsigned spin = 0;; ++spin) {
                    bool ok = true;
#pragma unroll
                    for (int k = 0; k < 4; ++k) { const int o = (k < 3 || lane < 32) ? k * 1024 + lane * 16 : lane * 16; v[k] = LD16(WS_E5 + rj * 4096 + o); ok &= TAGOK(v[k]); }
                    SPIN_END();
                }
                const float l0 = lane < 32 ? asf(v[3].x) : 0.f, l1 = lane < 32 ? asf(v[3].z) : 0.f;
                float a0 = (asf(v[0].x) + asf(v[1].x)) + (asf(v[2].x) + l0);
                float a1 = (asf(v[0].z) + asf(v[1].z)) + (asf(v[2].z) + l1);
                a0 = sum_hi4(a0); a1 = sum_hi4(a1);
                const float y0 = rbf(rbf(a0) + bf_lo(b2pair)), y1 = rbf(rbf(a1) + bf_hi(b2pair));
                if (lane < 4) *(unsigned*)(out + rc) = cvtpk(h0 + y0, h1 + y1);
                if (lane == 0 && rj == 0) __hip_atomic_store((gu32*)(ws + WS_EPOCH), tag, RLX_AGENT);
            }
            STAMP(9);
        }
        if (lane == 0) { STAMPOUT(0, 3); STAMPOUT(8, 10); }
    }
}

extern "C" void kernel_launch(void* const* d_in, const int* in_sizes, int n_in, void* d_out, int out_size, void* d_ws, size_t ws_size, hipStream_t stream) {
    (void)in_sizes; (void)n_in; (void)out_size; (void)ws_size;
    static bool attr_set = false;
    if (!attr_set) { (void)hipFuncSetAttribute((const void*)fused_block, hipFuncAttributeMaxDynamicSharedMemorySize, DYN_LDS); attr_set = true; }
    fused_block<<<NWG, NTHR, DYN_LDS, stream>>>((const u16*)d_in[0], (const u16*)d_in[1], (const u16*)d_in[2], (const u16*)d_in[3], (const u16*)d_in[4],
                                          (const u16*)d_in[6], (const u16*)d_in[7], (const u16*)d_in[8], (const u16*)d_in[9], (u16*)d_out, (unsigned char*)d_ws);
}
```

```cpp
#include <hip/hip_runtime.h>
#include <stdint.h>
#define DM 8
#define DELAY(n) do { for (int i_ = 0; i_ < (n); ++i_) __builtin_amdgcn_s_sleep(4); } while (0)
#define VMODE 0

typedef unsigned short u16;
typedef unsigned v4u __attribute__((ext_vector_type(4)));
typedef unsigned v2u __attribute__((ext_vector_type(2)));
typedef float v4f __attribute__((ext_vector_type(4)));
typedef float v2f __attribute__((ext_vector_type(2)));
typedef __bf16 bf16x2 __attribute__((ext_vector_type(2)));
typedef __bf16 bf16x8 __attribute__((ext_vector_type(8)));
#define GAS __attribute__((address_space(1)))
typedef GAS unsigned long long gu64;
typedef GAS unsigned gu32;
#define RLX_AGENT __ATOMIC_RELAXED, __HIP_MEMORY_SCOPE_AGENT

constexpr int SK = 65536, WIN = 4096, FF = 1792;
constexpr int NWG = 256, NTHR = 576;
constexpr unsigned WS_EPOCH = 0;
constexpr unsigned WS_E1 = 4096;
constexpr unsigned WS_E2O = WS_E1 + 8 * 1024 * 8;
constexpr unsigned WS_E2ML = WS_E2O + 8 * 8 * 8 * 32 * 8 * 8;
constexpr unsigned WS_E3A = WS_E2ML + 8 * 4 * 32 * 2 * 8;
constexpr unsigned WS_E3B = WS_E3A + 64 * 64 * 8 * 8;
constexpr unsigned WS_E5 = WS_E3B + 8 * 256 * 8;
constexpr unsigned WS_END = WS_E5 + 64 * 64 * 8 * 8;
constexpr unsigned SPIN_CAP = 1u << 22;

__device__ __forceinline__ float asf(unsigned u) { return __builtin_bit_cast(float, u); }
__device__ __forceinline__ unsigned asu(float f) { return __builtin_bit_cast(unsigned, f); }
__device__ __forceinline__ float bf_lo(unsigned u) { return __builtin_bit_cast(float, u << 16); }
__device__ __forceinline__ float bf_hi(unsigned u) { return __builtin_bit_cast(float, u & 0xffff0000u); }
__device__ __forceinline__ unsigned cvtpk(float lo, float hi) {
    const bf16x2 v = {(__bf16)lo, (__bf16)hi};
    return __builtin_bit_cast(unsigned, v);
}
__device__ __forceinline__ float rbf(float f) { return bf_lo(cvtpk(f, f)); }
template <int CTRL> __device__ __forceinline__ float dpp(float x) {
    return __builtin_bit_cast(float, __builtin_amdgcn_mov_dpp(__builtin_bit_cast(int, x), CTRL, 0xf, 0xf, true));
}
constexpr int XOR1 = 0xB1, XOR2 = 0x4E, XOR7 = 0x141, ROR4 = 0x124, ROR8 = 0x128;
__device__ __forceinline__ float swap32_add(float a, float b) {
    auto s = __builtin_amdgcn_permlane32_swap(asu(a), asu(b), false, false);
    return asf(s[0]) + asf(s[1]);
}
__device__ __forceinline__ float swap16_add(float a, float b) {
    auto s = __builtin_amdgcn_permlane16_swap(asu(a), asu(b), false, false);
    return asf(s[0]) + asf(s[1]);
}
__device__ __forceinline__ float swap32_max(float a) {
    auto s = __builtin_amdgcn_permlane32_swap(asu(a), asu(a), false, false);
    return fmaxf(asf(s[0]), asf(s[1]));
}
__device__ __forceinline__ float swap16_max(float a) {
    auto s = __builtin_amdgcn_permlane16_swap(asu(a), asu(a), false, false);
    return fmaxf(asf(s[0]), asf(s[1]));
}
__device__ __forceinline__ float row_sum(float v) { v += dpp<XOR1>(v); v += dpp<XOR2>(v); v += dpp<ROR4>(v); v += dpp<ROR8>(v); return v; }
__device__ __forceinline__ float row_max(float v) { v = fmaxf(v, dpp<XOR1>(v)); v = fmaxf(v, dpp<XOR2>(v)); v = fmaxf(v, dpp<ROR4>(v)); v = fmaxf(v, dpp<ROR8>(v)); return v; }
__device__ __forceinline__ float wave_sum(float v) { v = row_sum(v); v = swap16_add(v, v); v = swap32_add(v, v); return v; }
__device__ __forceinline__ float wave_max(float v) { v = row_max(v); v = swap16_max(v); v = swap32_max(v); return v; }
__device__ __forceinline__ float sum_hi4(float v) {
    v += dpp<ROR4>(v); v += dpp<ROR8>(v);
    v = swap16_add(v, v); v = swap32_add(v, v);
    return v;
}
__device__ __forceinline__ float dot2(unsigned a, unsigned b, float acc) {
    return __builtin_amdgcn_fdot2_f32_bf16(__builtin_bit_cast(bf16x2, a), __builtin_bit_cast(bf16x2, b), acc, false);
}
__device__ __forceinline__ unsigned plo(unsigned a, unsigned b) { return __builtin_amdgcn_perm(b, a, 0x05040100u); }
__device__ __forceinline__ unsigned phi(unsigned a, unsigned b) { return __builtin_amdgcn_perm(b, a, 0x07060302u); }
__device__ __forceinline__ void dot2x8(float (&a)[8], unsigned sp, const v4u& A, const v4u& B) {
    a[0] = dot2(plo(A.x, B.x), sp, a[0]); a[1] = dot2(phi(A.x, B.x), sp, a[1]);
    a[2] = dot2(plo(A.y, B.y), sp, a[2]); a[3] = dot2(phi(A.y, B.y), sp, a[3]);
    a[4] = dot2(plo(A.z, B.z), sp, a[4]); a[5] = dot2(phi(A.z, B.z), sp, a[5]);
    a[6] = dot2(plo(A.w, B.w), sp, a[6]); a[7] = dot2(phi(A.w, B.w), sp, a[7]);
}
#define BAR() asm volatile("s_waitcnt lgkmcnt(0)\n\ts_barrier" ::: "memory")
#define STAMP(i) do { } while (0)
#define STAMPOUT(lo, hi) do { } while (0)
#define SB() do { asm volatile("" ::: "memory"); __builtin_amdgcn_sched_barrier(0); } while (0)
#define LDG(p) __builtin_nontemporal_load((const v4u*)(p))
#define LD16(off) __builtin_amdgcn_raw_buffer_load_b128(rs, (off), 0, 16)
#define ST16(off, a, b) __builtin_amdgcn_raw_buffer_store_b128(v4u{(a), tag, (b), tag}, rs, (off), 0, 16)
#define TAGOK(v) (((v).y == tag) & ((v).w == tag))
#define SPIN_END() if (__all(ok) || spin > SPIN_CAP) break; asm volatile("" ::: "memory")

__device__ __forceinline__ void st_gran(unsigned char* ws, unsigned off, unsigned tag, unsigned val) {
    __hip_atomic_store((gu64*)(ws + off), ((unsigned long long)tag << 32) | val, RLX_AGENT);
}

__device__ __forceinline__ void ln_stats(const float (&v)[8], float& m, float& rsd) {
    float s = ((v[0] + v[1]) + (v[2] + v[3])) + ((v[4] + v[5]) + (v[6] + v[7]));
    s = wave_sum(s);
    const float mean = s * (1.f / 512.f);
    float q = 0.f;
#pragma unroll
    for (int i = 0; i < 8; ++i) { const float d = v[i] - mean; q = fmaf(d, d, q); }
    q = wave_sum(q);
    m = rbf(mean);
    rsd = __builtin_amdgcn_rcpf(rbf(__builtin_amdgcn_sqrtf(rbf(rbf(q * (1.f / 512.f)) + 1e-5f))));
}

struct Lds {
    float big[8 * 512];
    float hn[512];
    unsigned qs[256];
    unsigned pb[8 * 32];
    float ml[4 * 8 * 2];
    float red1[8 * 32];
    float red4[32 * 8];
    unsigned tag;
};

__global__ void __launch_bounds__(NTHR) fused_block(const u16* __restrict__ x, const u16* __restrict__ kc, const u16* __restrict__ vc,
                                                     const u16* __restrict__ wq, const u16* __restrict__ wo, const u16* __restrict__ w1,
                                                     const u16* __restrict__ b1, const u16* __restrict__ w2, const u16* __restrict__ b2,
                                                     u16* __restrict__ out, unsigned char* ws) {
    __shared__ __attribute__((aligned(16))) Lds L;
    const int tid = threadIdx.x, lane = tid & 63, wave = __builtin_amdgcn_readfirstlane(tid >> 6);
    const int b = blockIdx.x, g = b & 7, s = b >> 3;
    const bool is_wo = s < 8, is_mlp = (s >= 8) & (s < 15), is_red = (s >= 16) & (s < 24);
    const __amdgpu_buffer_rsrc_t rs = __builtin_amdgcn_make_buffer_rsrc(ws, 0, WS_END, 0x00020000);
    STAMP(0);

    if (wave < 8) {
        const int r = lane >> 4, c = lane & 15;
        const int r0 = 256 * (s & 1) + (tid >> 2), hf = tid & 3;
        const u16* wqp = wq + (size_t)r0 * 4096 + 512 * g + 32 * (s >> 1) + 8 * hf;
        const size_t kvrow = (size_t)g * SK + (SK - WIN) + 128 * s + 16 * wave;
        const u16* kp = kc + (kvrow + c) * 128 + 8 * r;
        const u16* vp = vc + kvrow * 128 + 2 * lane;
        SB();
        const v4u xv = *(const v4u*)(x + 8 * lane);
        const unsigned xr0 = x[r0], xr1 = x[r0 + 128];
        SB();
        const v4u q0 = LDG(wqp), q1 = LDG(wqp + (size_t)128 * 4096);
        SB();
        asm volatile("s_barrier" ::: "memory");
        v4u K[4];
#pragma unroll
        for (int i = 0; i < 4; ++i) K[i] = LDG(kp + i * 32);
        SB();
        {
            float xf[8] = {bf_lo(xv.x), bf_hi(xv.x), bf_lo(xv.y), bf_hi(xv.y), bf_lo(xv.z), bf_hi(xv.z), bf_lo(xv.w), bf_hi(xv.w)};
            float m, rsd;
            ln_stats(xf, m, rsd);
            const unsigned np = cvtpk(rbf(bf_lo(xr0) - m) * rsd, rbf(bf_lo(xr1) - m) * rsd);
            float a[8] = {0.f, 0.f, 0.f, 0.f, 0.f, 0.f, 0.f, 0.f};
            dot2x8(a, np, q0, q1);
            float u[4];
#pragma unroll
            for (int k = 0; k < 4; ++k) u[k] = swap32_add(a[k], a[k + 4]);
            const float z0 = swap16_add(u[0], u[2]), z1 = swap16_add(u[1], u[3]);
            float t0 = z0 + dpp<ROR4>(z0), t1 = z1 + dpp<ROR4>(z1);
            t0 += dpp<ROR8>(t0); t1 += dpp<ROR8>(t1);
            if ((lane & 12) == 0) *(v2f*)&L.red1[wave * 32 + 8 * (lane & 3) + 2 * ((lane >> 4) & 1) + 4 * (lane >> 5)] = v2f{t0, t1};
        }
        SB();
        STAMP(11);
        BAR();
        BAR();
        unsigned V[16];
#pragma unroll
        for (int i = 0; i < 16; ++i) V[i] = __builtin_nontemporal_load((const unsigned*)(vp + i * 128));
        SB();
        BAR();
        {
            v4f sacc = {0.f, 0.f, 0.f, 0.f};
#pragma unroll
            for (int ks = 0; ks < 4; ++ks) {
                const v4u qa = *(const v4u*)&L.qs[(c >> 2) * 64 + 16 * ks + 4 * r];
                sacc = __builtin_amdgcn_mfma_f32_16x16x32_bf16(__builtin_bit_cast(bf16x8, qa), __builtin_bit_cast(bf16x8, K[ks]), sacc, 0, 0, 0);
            }
            const float sc = rbf(rbf(sacc[0]) * 0.08838834764831845f);
            const float m = row_max(sc);
            const float p = __expf(sc - m);
            const float l = row_sum(p);
            const unsigned pp = cvtpk(p, dpp<XOR1>(p));
            if ((c & 1) == 0) L.pb[wave * 32 + r * 8 + (c >> 1)] = pp;
            if (c == 0) *(v2f*)&L.ml[(r * 8 + wave) * 2] = v2f{m, l};
            asm volatile("s_waitcnt lgkmcnt(0)" ::: "memory");
            v4u P[8];
#pragma unroll
            for (int i = 0; i < 8; ++i) P[i] = *(const v4u*)&L.pb[wave * 32 + 4 * i];
            float o[4][2];
#pragma unroll
            for (int h = 0; h < 4; ++h) { o[h][0] = 0.f; o[h][1] = 0.f; }
#pragma unroll
            for (int kp2 = 0; kp2 < 8; ++kp2) {
                const unsigned lo = plo(V[2 * kp2], V[2 * kp2 + 1]), hi = phi(V[2 * kp2], V[2 * kp2 + 1]);
#pragma unroll
                for (int h = 0; h < 4; ++h) { const unsigned pw = P[2 * h + (kp2 >> 2)][kp2 & 3]; o[h][0] = dot2(lo, pw, o[h][0]); o[h][1] = dot2(hi, pw, o[h][1]); }
            }
#pragma unroll
            for (int h = 0; h < 4; ++h) *(v2f*)&L.big[wave * 512 + h * 128 + 2 * lane] = v2f{o[h][0], o[h][1]};
        }
        BAR();
        const unsigned tag = L.tag;
        if (wave < 4) {
            const int h = wave, d = 2 * lane;
            const v4f ma = *(const v4f*)&L.ml[h * 16], mb = *(const v4f*)&L.ml[h * 16 + 4], mc = *(const v4f*)&L.ml[h * 16 + 8], md = *(const v4f*)&L.ml[h * 16 + 12];
            const float mw[8] = {ma.x, ma.z, mb.x, mb.z, mc.x, mc.z, md.x, md.z};
            const float lw[8] = {ma.y, ma.w, mb.y, mb.w, mc.y, mc.w, md.y, md.w};
            float M = mw[0];
#pragma unroll
            for (int w = 1; w < 8; ++w) M = fmaxf(M, mw[w]);
            float oa = 0.f, ob = 0.f, Ls = 0.f;
#pragma unroll
            for (int w = 0; w < 8; ++w) { const float e = __expf(mw[w] - M); const v2f t = *(const v2f*)&L.big[w * 512 + 2 * tid]; oa = fmaf(e, t.x, oa); ob = fmaf(e, t.y, ob); Ls = fmaf(e, lw[w], Ls); }
            ST16(WS_E2O + ((((g * 8 + 2 * h + (d >> 6)) * 8 + ((d >> 3) & 7)) * 32 + s) * 8 + (d & 7)) * 8, asu(oa), asu(ob));
            if (lane == 0) ST16(WS_E2ML + ((g * 4 + h) * 32 + s) * 16, asu(M), asu(Ls));
        }
        STAMP(3);
        if (!(is_wo | is_mlp)) { if (tid == 0) { STAMPOUT(3, 8); STAMPOUT(10, 12); } return; }
        BAR();
        v4u R[8];
        if (is_wo) {
            const u16* rp = wo + (size_t)(512 * g + 64 * s + 8 * wave) * 512 + 8 * lane;
#pragma unroll
            for (int i = 0; i < 8; ++i) R[i] = LDG(rp + i * 512);
            SB();
            v4u v[2], mlv;
            for (unsigned spin = 0;; ++spin) {
                bool ok = true;
#pragma unroll
                for (int k = 0; k < 2; ++k) { v[k] = LD16(WS_E2O + ((g * 8 + s) * 8 + wave) * 2048 + k * 1024 + lane * 16); ok &= TAGOK(v[k]); }
                mlv = LD16(WS_E2ML + (g * 4 + (s >> 1)) * 512 + (lane & 31) * 16);
                ok &= TAGOK(mlv);
                SPIN_END();
            }
            const float ms = asf(mlv.x), ls = asf(mlv.z);
            const float M = wave_max(ms);
            const float wgt = __expf(ms - M);
            const float rL = __builtin_amdgcn_rcpf(0.5f * wave_sum(wgt * ls));
            const float wa = __shfl(wgt, lane >> 2), wb = __shfl(wgt, (lane >> 2) + 16);
            float a0 = fmaf(wb, asf(v[1].x), wa * asf(v[0].x)), a1 = fmaf(wb, asf(v[1].z), wa * asf(v[0].z));
            const unsigned op = cvtpk(sum_hi4(a0) * rL, sum_hi4(a1) * rL);
            float acc[8] = {0.f, 0.f, 0.f, 0.f, 0.f, 0.f, 0.f, 0.f};
#pragma unroll
            for (int i = 0; i < 4; ++i) dot2x8(acc, __builtin_amdgcn_readlane(op, i), R[2 * i], R[2 * i + 1]);
            STAMP(4);
            float* dst = &L.big[wave * 512 + 8 * lane];
            *(v4f*)dst = v4f{acc[0], acc[1], acc[2], acc[3]};
            *(v4f*)(dst + 4) = v4f{acc[4], acc[5], acc[6], acc[7]};
            BAR();
            if (wave < 4) {
                float s0 = 0.f, s1 = 0.f;
#pragma unroll
                for (int w = 0; w < 8; ++w) { const v2f t = *(const v2f*)&L.big[w * 512 + 2 * tid]; s0 += t.x; s1 += t.y; }
                ST16(WS_E3A + (((tid >> 2) * 64 + (g * 8 + s)) * 8 + 2 * (tid & 3)) * 8, asu(s0), asu(s1));
            }
            STAMP(5);
        } else {
            const int j = g * 7 + (s - 8);
            const u16* rp0 = w1 + (size_t)(64 * wave + c) * FF + 32 * j + 8 * r;
            const u16* rp1 = w2 + (size_t)(32 * j + 4 * wave) * 512 + 8 * lane;
            DELAY(DM);
            SB();
#pragma unroll
            for (int i = 0; i < 4; ++i) R[i] = LDG(rp0 + (size_t)i * 16 * FF);
#pragma unroll
            for (int i = 0; i < 4; ++i) R[4 + i] = LDG(rp1 + i * 512);
            const unsigned b1v = b1[32 * j + 4 * wave + (lane & 3)];
            SB();
            BAR();
            STAMP(6);
            float hn[4];
#pragma unroll
            for (int i = 0; i < 4; ++i) hn[i] = L.hn[64 * wave + 16 * i + c];
            float xs[8] = {0.f, 0.f, 0.f, 0.f, 0.f, 0.f, 0.f, 0.f};
            dot2x8(xs, cvtpk(hn[0], hn[1]), R[0], R[1]);
            dot2x8(xs, cvtpk(hn[2], hn[3]), R[2], R[3]);
#define TR_STEP(HALF, CTRL, BIT) _Pragma("unroll") for (int h = 0; h < (HALF); ++h) { \
            const float lo_ = xs[h], hi_ = xs[h + (HALF)]; const float t1_ = lo_ + dpp<CTRL>(lo_), t2_ = hi_ + dpp<CTRL>(hi_); \
            xs[h] = (c & (BIT)) ? t2_ : t1_; }
            TR_STEP(4, ROR8, 8) TR_STEP(2, XOR7, 4) TR_STEP(1, XOR2, 2)
#undef TR_STEP
            const float rr = xs[0] + dpp<XOR1>(xs[0]);
            if ((c & 1) == 0) L.red4[(8 * r + ((c >> 3) & 1) * 4 + ((c >> 2) & 1) * 2 + ((c >> 1) & 1)) * 8 + wave] = rr;
            BAR();
            const v4f ra = *(const v4f*)&L.red4[(4 * wave + (lane & 3)) * 8], rb = *(const v4f*)&L.red4[(4 * wave + (lane & 3)) * 8 + 4];
            const float sum = ((ra.x + ra.y) + (ra.z + ra.w)) + ((rb.x + rb.y) + (rb.z + rb.w));
            const float u = rbf(rbf(sum) + bf_lo(b1v));
            const float z = 0.7978845608028654f * (u + 0.044715f * u * u * u);
            const float th = 1.f - 2.f * __builtin_amdgcn_rcpf(__expf(2.f * z) + 1.f);
            const float gl = 0.5f * u * (1.f + th);
            const unsigned gp = cvtpk(gl, dpp<XOR1>(gl));
            float acc[8] = {0.f, 0.f, 0.f, 0.f, 0.f, 0.f, 0.f, 0.f};
            dot2x8(acc, __builtin_amdgcn_readlane(gp, 0), R[4], R[5]);
            dot2x8(acc, __builtin_amdgcn_readlane(gp, 2), R[6], R[7]);
            float* dst = &L.big[wave * 512 + 8 * lane];
            *(v4f*)dst = v4f{acc[0], acc[1], acc[2], acc[3]};
            *(v4f*)(dst + 4) = v4f{acc[4], acc[5], acc[6], acc[7]};
            BAR();
            if (wave < 4) {
                float s0 = 0.f, s1 = 0.f;
#pragma unroll
                for (int w = 0; w < 8; ++w) { const v2f t = *(const v2f*)&L.big[w * 512 + 2 * tid]; s0 += t.x; s1 += t.y; }
                ST16(WS_E5 + (((tid >> 2) * 64 + j) * 8 + 2 * (tid & 3)) * 8, asu(s0), asu(s1));
            }
            STAMP(7);
        }
        if (tid == 0) { STAMPOUT(3, 8); STAMPOUT(10, 12); }
        return;
    }
    {
        const unsigned tag = __hip_atomic_load((gu32*)(ws + WS_EPOCH), RLX_AGENT) + 1u;
        const int rj = g * 8 + (s - 16);
        const int rc = is_red ? 8 * rj + 2 * (lane & 3) : 0;
        const unsigned xpair = *(const unsigned*)(x + rc);
        const unsigned b2pair = *(const unsigned*)(b2 + rc);
        asm volatile("s_barrier" ::: "memory");
        BAR();
        STAMP(1);
        if (lane == 16) L.tag = tag;
        if (lane < 16) {
            float a0 = 0.f, a1 = 0.f;
#pragma unroll
            for (int w = 0; w < 8; ++w) { const v2f t = *(const v2f*)&L.red1[w * 32 + 2 * lane]; a0 += t.x; a1 += t.y; }
            ST16(WS_E1 + ((g * 2 + (s & 1)) * 512 + 32 * (s >> 1) + 2 * lane) * 8, asu(a0), asu(a1));
        }
        BAR();
        {
            v4u v[8];
            for (unsigned spin = 0;; ++spin) {
                bool ok = true;
#pragma unroll
                for (int k = 0; k < 8; ++k) { v[k] = LD16(WS_E1 + g * 8192 + k * 1024 + lane * 16); ok &= TAGOK(v[k]); }
                SPIN_END();
            }
#pragma unroll
            for (int k = 0; k < 4; ++k) L.qs[64 * k + lane] = cvtpk(asf(v[k].x) + asf(v[k + 4].x), asf(v[k].z) + asf(v[k + 4].z));
        }
        STAMP(2);
        BAR();
        if (is_mlp) {
            BAR();
            BAR();
            v4u v[2];
            for (unsigned spin = 0;; ++spin) {
                bool ok = true;
#pragma unroll
                for (int k = 0; k < 2; ++k) { v[k] = LD16(WS_E3B + g * 2048 + k * 1024 + lane * 16); ok &= TAGOK(v[k]); }
                SPIN_END();
            }
            float hf[8] = {bf_lo(v[0].x), bf_hi(v[0].x), bf_lo(v[0].z), bf_hi(v[0].z), bf_lo(v[1].x), bf_hi(v[1].x), bf_lo(v[1].z), bf_hi(v[1].z)};
            float m, rsd;
            ln_stats(hf, m, rsd);
            *(v4f*)&L.hn[4 * lane] = v4f{rbf(hf[0] - m) * rsd, rbf(hf[1] - m) * rsd, rbf(hf[2] - m) * rsd, rbf(hf[3] - m) * rsd};
            *(v4f*)&L.hn[256 + 4 * lane] = v4f{rbf(hf[4] - m) * rsd, rbf(hf[5] - m) * rsd, rbf(hf[6] - m) * rsd, rbf(hf[7] - m) * rsd};
            BAR();
        }
        if (is_red) {
            BAR();
            float h0, h1;
            {
                v4u v[4];
                for (unsigned spin = 0;; ++spin) {
                    bool ok = true;
#pragma unroll
                    for (int k = 0; k < 4; ++k) { v[k] = LD16(WS_E3A + rj * 4096 + k * 1024 + lane * 16); ok &= TAGOK(v[k]); }
                    SPIN_END();
                }
                float a0 = (asf(v[0].x) + asf(v[1].x)) + (asf(v[2].x) + asf(v[3].x));
                float a1 = (asf(v[0].z) + asf(v[1].z)) + (asf(v[2].z) + asf(v[3].z));
                a0 = sum_hi4(a0); a1 = sum_hi4(a1);
                const unsigned hp = cvtpk(bf_lo(xpair) + rbf(a0), bf_hi(xpair) + rbf(a1));
                h0 = bf_lo(hp); h1 = bf_hi(hp);
                if (lane < 32) st_gran(ws, WS_E3B + ((lane >> 2) * 256 + 4 * rj + (lane & 3)) * 8, tag, hp);
            }
            STAMP(8);
            {
                v4u v[4];
                for (unsigned spin = 0;; ++spin) {
                    bool ok = true;
#pragma unroll
                    for (int k = 0; k < 4; ++k) { const int o = (k < 3 || lane < 32) ? k * 1024 + lane * 16 : lane * 16; v[k] = LD16(WS_E5 + rj * 4096 + o); ok &= TAGOK(v[k]); }
                    SPIN_END();
                }
                const float l0 = lane < 32 ? asf(v[3].x) : 0.f, l1 = lane < 32 ? asf(v[3].z) : 0.f;
                float a0 = (asf(v[0].x) + asf(v[1].x)) + (asf(v[2].x) + l0);
                float a1 = (asf(v[0].z) + asf(v[1].z)) + (asf(v[2].z) + l1);
                a0 = sum_hi4(a0); a1 = sum_hi4(a1);
                const float y0 = rbf(rbf(a0) + bf_lo(b2pair)), y1 = rbf(rbf(a1) + bf_hi(b2pair));
                if (lane < 4) *(unsigned*)(out + rc) = cvtpk(h0 + y0, h1 + y1);
                if (lane == 0 && rj == 0) __hip_atomic_store((gu32*)(ws + WS_EPOCH), tag, RLX_AGENT);
            }
            STAMP(9);
        }
        if (lane == 0) { STAMPOUT(0, 3); STAMPOUT(8, 10); }
    }
}

extern "C" void kernel_launch(void* const* d_in, const int* in_sizes, int n_in, void* d_out, int out_size, void* d_ws, size_t ws_size, hipStream_t stream) {
    (void)in_sizes; (void)n_in; (void)out_size; (void)ws_size;
    fused_block<<<NWG, NTHR, 0, stream>>>((const u16*)d_in[0], (const u16*)d_in[1], (const u16*)d_in[2], (const u16*)d_in[3], (const u16*)d_in[4],
                                          (const u16*)d_in[6], (const u16*)d_in[7], (const u16*)d_in[8], (const u16*)d_in[9], (u16*)d_out, (unsigned char*)d_ws);
}
```

```cpp
#include <hip/hip_runtime.h>
#include <stdint.h>

typedef unsigned short u16;
typedef unsigned v4u __attribute__((ext_vector_type(4)));
typedef unsigned v2u __attribute__((ext_vector_type(2)));
typedef float v4f __attribute__((ext_vector_type(4)));
typedef float v2f __attribute__((ext_vector_type(2)));
typedef __bf16 bf16x2 __attribute__((ext_vector_type(2)));
typedef __bf16 bf16x8 __attribute__((ext_vector_type(8)));
#define GAS __attribute__((address_space(1)))
typedef GAS unsigned long long gu64;
typedef GAS unsigned gu32;
#define RLX_AGENT __ATOMIC_RELAXED, __HIP_MEMORY_SCOPE_AGENT

constexpr int SK = 65536, WIN = 4096, FF = 1792;
constexpr int NWG = 256, NTHR = 576;
constexpr unsigned WS_EPOCH = 0;
constexpr unsigned WS_E1 = 4096;
constexpr unsigned WS_E2O = WS_E1 + 8 * 1024 * 8;
constexpr unsigned WS_E2ML = WS_E2O + 8 * 8 * 8 * 32 * 8 * 8;
constexpr unsigned WS_E3A = WS_E2ML + 8 * 4 * 32 * 2 * 8;
constexpr unsigned WS_E3B = WS_E3A + 64 * 64 * 8 * 8;
constexpr unsigned WS_E5 = WS_E3B + 8 * 256 * 8;
constexpr unsigned WS_END = WS_E5 + 64 * 64 * 8 * 8;
constexpr unsigned SPIN_CAP = 1u << 22;

__device__ __forceinline__ float asf(unsigned u) { return __builtin_bit_cast(float, u); }
__device__ __forceinline__ unsigned asu(float f) { return __builtin_bit_cast(unsigned, f); }
__device__ __forceinline__ float bf_lo(unsigned u) { return __builtin_bit_cast(float, u << 16); }
__device__ __forceinline__ float bf_hi(unsigned u) { return __builtin_bit_cast(float, u & 0xffff0000u); }
__device__ __forceinline__ unsigned cvtpk(float lo, float hi) {
    const bf16x2 v = {(__bf16)lo, (__bf16)hi};
    return __builtin_bit_cast(unsigned, v);
}
__device__ __forceinline__ float rbf(float f) { return bf_lo(cvtpk(f, f)); }
template <int CTRL> __device__ __forceinline__ float dpp(float x) {
    return __builtin_bit_cast(float, __builtin_amdgcn_mov_dpp(__builtin_bit_cast(int, x), CTRL, 0xf, 0xf, true));
}
constexpr int XOR1 = 0xB1, XOR2 = 0x4E, XOR7 = 0x141, ROR4 = 0x124, ROR8 = 0x128;
__device__ __forceinline__ float swap32_add(float a, float b) {
    auto s = __builtin_amdgcn_permlane32_swap(asu(a), asu(b), false, false);
    return asf(s[0]) + asf(s[1]);
}
__device__ __forceinline__ float swap16_add(float a, float b) {
    auto s = __builtin_amdgcn_permlane16_swap(asu(a), asu(b), false, false);
    return asf(s[0]) + asf(s[1]);
}
__device__ __forceinline__ float swap32_max(float a) {
    auto s = __builtin_amdgcn_permlane32_swap(asu(a), asu(a), false, false);
    return fmaxf(asf(s[0]), asf(s[1]));
}
__device__ __forceinline__ float swap16_max(float a) {
    auto s = __builtin_amdgcn_permlane16_swap(asu(a), asu(a), false, false);
    return fmaxf(asf(s[0]), asf(s[1]));
}
__device__ __forceinline__ float row_sum(float v) { v += dpp<XOR1>(v); v += dpp<XOR2>(v); v += dpp<ROR4>(v); v += dpp<ROR8>(v); return v; }
__device__ __forceinline__ float row_max(float v) { v = fmaxf(v, dpp<XOR1>(v)); v = fmaxf(v, dpp<XOR2>(v)); v = fmaxf(v, dpp<ROR4>(v)); v = fmaxf(v, dpp<ROR8>(v)); return v; }
__device__ __forceinline__ float wave_sum(float v) { v = row_sum(v); v = swap16_add(v, v); v = swap32_add(v, v); return v; }
__device__ __forceinline__ float wave_max(float v) { v = row_max(v); v = swap16_max(v); v = swap32_max(v); return v; }
__device__ __forceinline__ float sum_hi4(float v) {
    v += dpp<ROR4>(v); v += dpp<ROR8>(v);
    v = swap16_add(v, v); v = swap32_add(v, v);
    return v;
}
__device__ __forceinline__ float dot2(unsigned a, unsigned b, float acc) {
    return __builtin_amdgcn_fdot2_f32_bf16(__builtin_bit_cast(bf16x2, a), __builtin_bit_cast(bf16x2, b), acc, false);
}
__device__ __forceinline__ unsigned plo(unsigned a, unsigned b) { return __builtin_amdgcn_perm(b, a, 0x05040100u); }
__device__ __forceinline__ unsigned phi(unsigned a, unsigned b) { return __builtin_amdgcn_perm(b, a, 0x07060302u); }
__device__ __forceinline__ void dot2x8(float (&a)[8], unsigned sp, const v4u& A, const v4u& B) {
    a[0] = dot2(plo(A.x, B.x), sp, a[0]); a[1] = dot2(phi(A.x, B.x), sp, a[1]);
    a[2] = dot2(plo(A.y, B.y), sp, a[2]); a[3] = dot2(phi(A.y, B.y), sp, a[3]);
    a[4] = dot2(plo(A.z, B.z), sp, a[4]); a[5] = dot2(phi(A.z, B.z), sp, a[5]);
    a[6] = dot2(plo(A.w, B.w), sp, a[6]); a[7] = dot2(phi(A.w, B.w), sp, a[7]);
}
#define BAR() asm volatile("s_waitcnt lgkmcnt(0)\n\ts_barrier" ::: "memory")
#define SB() do { asm volatile("" ::: "memory"); __builtin_amdgcn_sched_barrier(0); } while (0)
#define LDG(p) __builtin_nontemporal_load((const v4u*)(p))
#define LD16(off) __builtin_amdgcn_raw_buffer_load_b128(rs, (off), 0, 16)
#define ST16(off, a, b) __builtin_amdgcn_raw_buffer_store_b128(v4u{(a), tag, (b), tag}, rs, (off), 0, 16)
#define TAGOK(v) (((v).y == tag) & ((v).w == tag))
#define SPIN_END() if (__all(ok) || spin > SPIN_CAP) break; asm volatile("" ::: "memory")

__device__ __forceinline__ void st_gran(unsigned char* ws, unsigned off, unsigned tag, unsigned val) {
    __hip_atomic_store((gu64*)(ws + off), ((unsigned long long)tag << 32) | val, RLX_AGENT);
}

__device__ __forceinline__ void ln_stats(const float (&v)[8], float& m, float& rsd) {
    float s = ((v[0] + v[1]) + (v[2] + v[3])) + ((v[4] + v[5]) + (v[6] + v[7]));
    s = wave_sum(s);
    const float mean = s * (1.f / 512.f);
    float q = 0.f;
#pragma unroll
    for (int i = 0; i < 8; ++i) { const float d = v[i] - mean; q = fmaf(d, d, q); }
    q = wave_sum(q);
    m = rbf(mean);
    rsd = __builtin_amdgcn_rcpf(rbf(__builtin_amdgcn_sqrtf(rbf(rbf(q * (1.f / 512.f)) + 1e-5f))));
}

struct Lds {
    float big[8 * 512];
    float hn[512];
    unsigned qs[256];
    unsigned pb[8 * 32];
    float ml[4 * 8 * 2];
    float red1[8 * 32];
    float red4[32 * 8];
    unsigned tag;
};

__global__ void __launch_bounds__(NTHR) fused_block(const u16* __restrict__ x, const u16* __restrict__ kc, const u16* __restrict__ vc,
                                                     const u16* __restrict__ wq, const u16* __restrict__ wo, const u16* __restrict__ w1,
                                                     const u16* __restrict__ b1, const u16* __restrict__ w2, const u16* __restrict__ b2,
                                                     u16* __restrict__ out, unsigned char* ws) {
    __shared__ __attribute__((aligned(16))) Lds L;
    const int tid = threadIdx.x, lane = tid & 63, wave = __builtin_amdgcn_readfirstlane(tid >> 6);
    const int b = blockIdx.x, g = b & 7, s = b >> 3;
    const bool is_wo = s < 8, is_mlp = (s >= 8) & (s < 15), is_red = (s >= 16) & (s < 24);
    const __amdgpu_buffer_rsrc_t rs = __builtin_amdgcn_make_buffer_rsrc(ws, 0, WS_END, 0x00020000);

    if (__builtin_expect(wave < 8, 1)) {
        const int r = lane >> 4, c = lane & 15;
        const int r0 = 256 * (s & 1) + (tid >> 2), hf = tid & 3;
        const u16* wqp = wq + (size_t)r0 * 4096 + 512 * g + 32 * (s >> 1) + 8 * hf;
        const size_t kvrow = (size_t)g * SK + (SK - WIN) + 128 * s + 16 * wave;
        const u16* kp = kc + (kvrow + c) * 128 + 8 * r;
        const u16* vp = vc + kvrow * 128 + 2 * lane;
        SB();
        const v4u xv = *(const v4u*)(x + 8 * lane);
        const unsigned xr0 = x[r0], xr1 = x[r0 + 128];
        SB();
        const v4u q0 = LDG(wqp), q1 = LDG(wqp + (size_t)128 * 4096);
        SB();
        asm volatile("s_barrier" ::: "memory");
        v4u K[4];
#pragma unroll
        for (int i = 0; i < 4; ++i) K[i] = LDG(kp + i * 32);
        SB();
        {
            float xf[8] = {bf_lo(xv.x), bf_hi(xv.x), bf_lo(xv.y), bf_hi(xv.y), bf_lo(xv.z), bf_hi(xv.z), bf_lo(xv.w), bf_hi(xv.w)};
            float m, rsd;
            ln_stats(xf, m, rsd);
            const unsigned np = cvtpk(rbf(bf_lo(xr0) - m) * rsd, rbf(bf_lo(xr1) - m) * rsd);
            float a[8] = {0.f, 0.f, 0.f, 0.f, 0.f, 0.f, 0.f, 0.f};
            dot2x8(a, np, q0, q1);
            float u[4];
#pragma unroll
            for (int k = 0; k < 4; ++k) u[k] = swap32_add(a[k], a[k + 4]);
            const float z0 = swap16_add(u[0], u[2]), z1 = swap16_add(u[1], u[3]);
            float t0 = z0 + dpp<ROR4>(z0), t1 = z1 + dpp<ROR4>(z1);
            t0 += dpp<ROR8>(t0); t1 += dpp<ROR8>(t1);
            if ((lane & 12) == 0) *(v2f*)&L.red1[wave * 32 + 8 * (lane & 3) + 2 * ((lane >> 4) & 1) + 4 * (lane >> 5)] = v2f{t0, t1};
        }
        SB();
        BAR();
        BAR();
        unsigned V[16];
#pragma unroll
        for (int i = 0; i < 16; ++i) V[i] = __builtin_nontemporal_load((const unsigned*)(vp + i * 128));
        SB();
        BAR();
        {
            v4f sacc = {0.f, 0.f, 0.f, 0.f};
#pragma unroll
            for (int ks = 0; ks < 4; ++ks) {
                const v4u qa = *(const v4u*)&L.qs[(c >> 2) * 64 + 16 * ks + 4 * r];
                sacc = __builtin_amdgcn_mfma_f32_16x16x32_bf16(__builtin_bit_cast(bf16x8, qa), __builtin_bit_cast(bf16x8, K[ks]), sacc, 0, 0, 0);
            }
            const float sc = rbf(rbf(sacc[0]) * 0.08838834764831845f);
            const float m = row_max(sc);
            const float p = __expf(sc - m);
            const float l = row_sum(p);
            const unsigned pp = cvtpk(p, dpp<XOR1>(p));
            if ((c & 1) == 0) L.pb[wave * 32 + r * 8 + (c >> 1)] = pp;
            if (c == 0) *(v2f*)&L.ml[(r * 8 + wave) * 2] = v2f{m, l};
            asm volatile("s_waitcnt lgkmcnt(0)" ::: "memory");
            v4u P[8];
#pragma unroll
            for (int i = 0; i < 8; ++i) P[i] = *(const v4u*)&L.pb[wave * 32 + 4 * i];
            float o[4][2];
#pragma unroll
            for (int h = 0; h < 4; ++h) { o[h][0] = 0.f; o[h][1] = 0.f; }
#pragma unroll
            for (int kp2 = 0; kp2 < 8; ++kp2) {
                const unsigned lo = plo(V[2 * kp2], V[2 * kp2 + 1]), hi = phi(V[2 * kp2], V[2 * kp2 + 1]);
#pragma unroll
                for (int h = 0; h < 4; ++h) { const unsigned pw = P[2 * h + (kp2 >> 2)][kp2 & 3]; o[h][0] = dot2(lo, pw, o[h][0]); o[h][1] = dot2(hi, pw, o[h][1]); }
            }
#pragma unroll
            for (int h = 0; h < 4; ++h) *(v2f*)&L.big[wave * 512 + h * 128 + 2 * lane] = v2f{o[h][0], o[h][1]};
        }
        BAR();
        const unsigned tag = L.tag;
        if (wave < 4) {
            const int h = wave, d = 2 * lane;
            const v4f ma = *(const v4f*)&L.ml[h * 16], mb = *(const v4f*)&L.ml[h * 16 + 4], mc = *(const v4f*)&L.ml[h * 16 + 8], md = *(const v4f*)&L.ml[h * 16 + 12];
            const float mw[8] = {ma.x, ma.z, mb.x, mb.z, mc.x, mc.z, md.x, md.z};
            const float lw[8] = {ma.y, ma.w, mb.y, mb.w, mc.y, mc.w, md.y, md.w};
            float M = mw[0];
#pragma unroll
            for (int w = 1; w < 8; ++w) M = fmaxf(M, mw[w]);
            float oa = 0.f, ob = 0.f, Ls = 0.f;
#pragma unroll
            for (int w = 0; w < 8; ++w) { const float e = __expf(mw[w] - M); const v2f t = *(const v2f*)&L.big[w * 512 + 2 * tid]; oa = fmaf(e, t.x, oa); ob = fmaf(e, t.y, ob); Ls = fmaf(e, lw[w], Ls); }
            ST16(WS_E2O + ((((g * 8 + 2 * h + (d >> 6)) * 8 + ((d >> 3) & 7)) * 32 + s) * 8 + (d & 7)) * 8, asu(oa), asu(ob));
            if (lane == 0) ST16(WS_E2ML + ((g * 4 + h) * 32 + s) * 16, asu(M), asu(Ls));
        }
        if (!(is_wo | is_mlp)) return;
        BAR();
        v4u R[8];
        if (__builtin_expect(is_wo, 1)) {
            const u16* rp = wo + (size_t)(512 * g + 64 * s + 8 * wave) * 512 + 8 * lane;
#pragma unroll
            for (int i = 0; i < 8; ++i) R[i] = LDG(rp + i * 512);
            SB();
            v4u v[2], mlv;
            for (unsigned spin = 0;; ++spin) {
                bool ok = true;
#pragma unroll
                for (int k = 0; k < 2; ++k) { v[k] = LD16(WS_E2O + ((g * 8 + s) * 8 + wave) * 2048 + k * 1024 + lane * 16); ok &= TAGOK(v[k]); }
                mlv = LD16(WS_E2ML + (g * 4 + (s >> 1)) * 512 + (lane & 31) * 16);
                ok &= TAGOK(mlv);
                SPIN_END();
            }
            const float ms = asf(mlv.x), ls = asf(mlv.z);
            const float M = wave_max(ms);
            const float wgt = __expf(ms - M);
            const float rL = __builtin_amdgcn_rcpf(0.5f * wave_sum(wgt * ls));
            const float wa = __shfl(wgt, lane >> 2), wb = __shfl(wgt, (lane >> 2) + 16);
            const float a0 = fmaf(wb, asf(v[1].x), wa * asf(v[0].x)), a1 = fmaf(wb, asf(v[1].z), wa * asf(v[0].z));
            const unsigned op = cvtpk(sum_hi4(a0) * rL, sum_hi4(a1) * rL);
            float acc[8] = {0.f, 0.f, 0.f, 0.f, 0.f, 0.f, 0.f, 0.f};
#pragma unroll
            for (int i = 0; i < 4; ++i) dot2x8(acc, __builtin_amdgcn_readlane(op, i), R[2 * i], R[2 * i + 1]);
            float* dst = &L.big[wave * 512 + 8 * lane];
            *(v4f*)dst = v4f{acc[0], acc[1], acc[2], acc[3]};
            *(v4f*)(dst + 4) = v4f{acc[4], acc[5], acc[6], acc[7]};
            BAR();
            if (wave < 4) {
                float s0 = 0.f, s1 = 0.f;
#pragma unroll
                for (int w = 0; w < 8; ++w) { const v2f t = *(const v2f*)&L.big[w * 512 + 2 * tid]; s0 += t.x; s1 += t.y; }
                ST16(WS_E3A + (((tid >> 2) * 64 + (g * 8 + s)) * 8 + 2 * (tid & 3)) * 8, asu(s0), asu(s1));
            }
        } else {
            const int j = g * 7 + (s - 8);
            const u16* rp0 = w1 + (size_t)(64 * wave + c) * FF + 32 * j + 8 * r;
            const u16* rp1 = w2 + (size_t)(32 * j + 4 * wave) * 512 + 8 * lane;
            for (int i = 0; i < 8; ++i) __builtin_amdgcn_s_sleep(4);
            SB();
#pragma unroll
            for (int i = 0; i < 4; ++i) R[i] = LDG(rp0 + (size_t)i * 16 * FF);
#pragma unroll
            for (int i = 0; i < 4; ++i) R[4 + i] = LDG(rp1 + i * 512);
            const unsigned b1v = b1[32 * j + 4 * wave + (lane & 3)];
            SB();
            BAR();
            float hn[4];
#pragma unroll
            for (int i = 0; i < 4; ++i) hn[i] = L.hn[64 * wave + 16 * i + c];
            float xs[8] = {0.f, 0.f, 0.f, 0.f, 0.f, 0.f, 0.f, 0.f};
            dot2x8(xs, cvtpk(hn[0], hn[1]), R[0], R[1]);
            dot2x8(xs, cvtpk(hn[2], hn[3]), R[2], R[3]);
#define TR_STEP(HALF, CTRL, BIT) _Pragma("unroll") for (int h = 0; h < (HALF); ++h) { \
            const float lo_ = xs[h], hi_ = xs[h + (HALF)]; const float t1_ = lo_ + dpp<CTRL>(lo_), t2_ = hi_ + dpp<CTRL>(hi_); \
            xs[h] = (c & (BIT)) ? t2_ : t1_; }
            TR_STEP(4, ROR8, 8) TR_STEP(2, XOR7, 4) TR_STEP(1, XOR2, 2)
#undef TR_STEP
            const float rr = xs[0] + dpp<XOR1>(xs[0]);
            if ((c & 1) == 0) L.red4[(8 * r + ((c >> 3) & 1) * 4 + ((c >> 2) & 1) * 2 + ((c >> 1) & 1)) * 8 + wave] = rr;
            BAR();
            const v4f ra = *(const v4f*)&L.red4[(4 * wave + (lane & 3)) * 8], rb = *(const v4f*)&L.red4[(4 * wave + (lane & 3)) * 8 + 4];
            const float sum = ((ra.x + ra.y) + (ra.z + ra.w)) + ((rb.x + rb.y) + (rb.z + rb.w));
            const float u = rbf(rbf(sum) + bf_lo(b1v));
            const float z = 0.7978845608028654f * (u + 0.044715f * u * u * u);
            const float th = 1.f - 2.f * __builtin_amdgcn_rcpf(__expf(2.f * z) + 1.f);
            const float gl = 0.5f * u * (1.f + th);
            const unsigned gp = cvtpk(gl, dpp<XOR1>(gl));
            float acc[8] = {0.f, 0.f, 0.f, 0.f, 0.f, 0.f, 0.f, 0.f};
            dot2x8(acc, __builtin_amdgcn_readlane(gp, 0), R[4], R[5]);
            dot2x8(acc, __builtin_amdgcn_readlane(gp, 2), R[6], R[7]);
            float* dst = &L.big[wave * 512 + 8 * lane];
            *(v4f*)dst = v4f{acc[0], acc[1], acc[2], acc[3]};
            *(v4f*)(dst + 4) = v4f{acc[4], acc[5], acc[6], acc[7]};
            BAR();
            if (wave < 4) {
                float s0 = 0.f, s1 = 0.f;
#pragma unroll
                for (int w = 0; w < 8; ++w) { const v2f t = *(const v2f*)&L.big[w * 512 + 2 * tid]; s0 += t.x; s1 += t.y; }
                ST16(WS_E5 + (((tid >> 2) * 64 + j) * 8 + 2 * (tid & 3)) * 8, asu(s0), asu(s1));
            }
        }
        return;
    }
    {
        const unsigned tag = __hip_atomic_load((gu32*)(ws + WS_EPOCH), RLX_AGENT) + 1u;
        const int rj = g * 8 + (s - 16);
        const int rc = is_red ? 8 * rj + 2 * (lane & 3) : 0;
        const unsigned xpair = *(const unsigned*)(x + rc);
        const unsigned b2pair = *(const unsigned*)(b2 + rc);
        asm volatile("s_barrier" ::: "memory");
        BAR();
        if (lane == 16) L.tag = tag;
        if (lane < 16) {
            float a0 = 0.f, a1 = 0.f;
#pragma unroll
            for (int w = 0; w < 8; ++w) { const v2f t = *(const v2f*)&L.red1[w * 32 + 2 * lane]; a0 += t.x; a1 += t.y; }
            ST16(WS_E1 + ((g * 2 + (s & 1)) * 512 + 32 * (s >> 1) + 2 * lane) * 8, asu(a0), asu(a1));
        }
        BAR();
        {
            v4u v[8];
            for (unsigned spin = 0;; ++spin) {
                bool ok = true;
#pragma unroll
                for (int k = 0; k < 8; ++k) { v[k] = LD16(WS_E1 + g * 8192 + k * 1024 + lane * 16); ok &= TAGOK(v[k]); }
                SPIN_END();
            }
#pragma unroll
            for (int k = 0; k < 4; ++k) L.qs[64 * k + lane] = cvtpk(asf(v[k].x) + asf(v[k + 4].x), asf(v[k].z) + asf(v[k + 4].z));
        }
        BAR();
        if (is_mlp) {
            BAR();
            BAR();
            v4u v[2];
            for (unsigned spin = 0;; ++spin) {
                bool ok = true;
#pragma unroll
                for (int k = 0; k < 2; ++k) { v[k] = LD16(WS_E3B + g * 2048 + k * 1024 + lane * 16); ok &= TAGOK(v[k]); }
                SPIN_END();
            }
            float hf[8] = {bf_lo(v[0].x), bf_hi(v[0].x), bf_lo(v[0].z), bf_hi(v[0].z), bf_lo(v[1].x), bf_hi(v[1].x), bf_lo(v[1].z), bf_hi(v[1].z)};
            float m, rsd;
            ln_stats(hf, m, rsd);
            *(v4f*)&L.hn[4 * lane] = v4f{rbf(hf[0] - m) * rsd, rbf(hf[1] - m) * rsd, rbf(hf[2] - m) * rsd, rbf(hf[3] - m) * rsd};
            *(v4f*)&L.hn[256 + 4 * lane] = v4f{rbf(hf[4] - m) * rsd, rbf(hf[5] - m) * rsd, rbf(hf[6] - m) * rsd, rbf(hf[7] - m) * rsd};
            BAR();
        }
        if (is_red) {
            BAR();
            float h0, h1;
            {
                v4u v[4];
                for (unsigned spin = 0;; ++spin) {
                    bool ok = true;
#pragma unroll
                    for (int k = 0; k < 4; ++k) { v[k] = LD16(WS_E3A + rj * 4096 + k * 1024 + lane * 16); ok &= TAGOK(v[k]); }
                    SPIN_END();
                }
                float a0 = (asf(v[0].x) + asf(v[1].x)) + (asf(v[2].x) + asf(v[3].x));
                float a1 = (asf(v[0].z) + asf(v[1].z)) + (asf(v[2].z) + asf(v[3].z));
                a0 = sum_hi4(a0); a1 = sum_hi4(a1);
                const unsigned hp = cvtpk(bf_lo(xpair) + rbf(a0), bf_hi(xpair) + rbf(a1));
                h0 = bf_lo(hp); h1 = bf_hi(hp);
                if (lane < 32) st_gran(ws, WS_E3B + ((lane >> 2) * 256 + 4 * rj + (lane & 3)) * 8, tag, hp);
            }
            {
                v4u v[4];
                for (unsigned spin = 0;; ++spin) {
                    bool ok = true;
#pragma unroll
                    for (int k = 0; k < 4; ++k) { const int o = (k < 3 || lane < 32) ? k * 1024 + lane * 16 : lane * 16; v[k] = LD16(WS_E5 + rj * 4096 + o); ok &= TAGOK(v[k]); }
                    SPIN_END();
                }
                const float l0 = lane < 32 ? asf(v[3].x) : 0.f, l1 = lane < 32 ? asf(v[3].z) : 0.f;
                float a0 = (asf(v[0].x) + asf(v[1].x)) + (asf(v[2].x) + l0);
                float a1 = (asf(v[0].z) + asf(v[1].z)) + (asf(v[2].z) + l1);
                a0 = sum_hi4(a0); a1 = sum_hi4(a1);
                const float y0 = rbf(rbf(a0) + bf_lo(b2pair)), y1 = rbf(rbf(a1) + bf_hi(b2pair));
                if (lane < 4) *(unsigned*)(out + rc) = cvtpk(h0 + y0, h1 + y1);
                if (lane == 0 && rj == 0) __hip_atomic_store((gu32*)(ws + WS_EPOCH), tag, RLX_AGENT);
            }
        }
    }
}

extern "C" void kernel_launch(void* const* d_in, const int* in_sizes, int n_in, void* d_out, int out_size, void* d_ws, size_t ws_size, hipStream_t stream) {
    (void)in_sizes; (void)n_in; (void)out_size; (void)ws_size;
    fused_block<<<NWG, NTHR, 0, stream>>>((const u16*)d_in[0], (const u16*)d_in[1], (const u16*)d_in[2], (const u16*)d_in[3], (const u16*)d_in[4],
                                          (const u16*)d_in[6], (const u16*)d_in[7], (const u16*)d_in[8], (const u16*)d_in[9], (u16*)d_out, (unsigned char*)d_ws);
}
```
